```python
import torch

def _softmax(x, axis=-1):
    return torch.softmax(x, dim=axis)

def _gelu(x):
    return torch.nn.functional.gelu(x, approximate='tanh')

def _silu(x):
    return torch.nn.functional.silu(x)

def _sigmoid(x):
    return torch.sigmoid(x)

def _layernorm(x, axis=-1, eps=1e-05):
    m = x.mean(dim=axis, keepdim=True)
    v = x.var(dim=axis, keepdim=True, unbiased=False)
    return (x - m) / torch.sqrt(v + eps)

def _scatter_add(tgt, idx, src, axis=0):
    return tgt.clone().index_add_(axis, idx, src)

def _segment_sum(src, seg, n):
    out = torch.zeros((n,) + src.shape[1:], dtype=src.dtype)
    return out.index_add_(0, seg, src)

def _pad(x, pw):
    flat = tuple((v for pair in reversed(pw) for v in pair))
    return torch.nn.functional.pad(x, flat)
kernel_name = 'recurrent_scan__spectral_gfnet__mixer_gmlp__prompt'

def setup_inputs():

    _w = lambda a: (lambda t: t.to(torch.bfloat16) if t.is_floating_point() else t)(torch.as_tensor(a))

    def _A_setup():
        B, S, D = (1, 2048, 448)
        Df = D // 2 + 1
        return {'x': _w((lambda *s: torch.randn(*s, dtype=torch.bfloat16) * 0.05)(B, S, D)), 'w_r': _w((lambda *s: torch.randn(*s, dtype=torch.bfloat16) * 0.2)(S, Df) * 0.02), 'w_i': _w((lambda *s: torch.randn(*s, dtype=torch.bfloat16) * 0.2)(S, Df) * 0.02)}
    _A_in = _A_setup()

    def _B_setup():
        B, S, D, F = (1, 2048, 448, 1792)
        Fh = F // 2
        return {'x': _w((lambda *s: torch.randn(*s, dtype=torch.bfloat16) * 0.05)(B, S, D)), 'w_in': _w((lambda *s: torch.randn(*s, dtype=torch.bfloat16) * 0.4)(D, 2 * Fh)), 'w_sp': _w(0.01 * (lambda *s: torch.randn(*s, dtype=torch.bfloat16) * 0.4)(S, S)), 'b_sp': _w(1.0 + 0.01 * (lambda *s: torch.randn(*s, dtype=torch.bfloat16) * 0.1)(S)), 'w_out': _w((lambda *s: torch.randn(*s, dtype=torch.bfloat16) * 0.1)(Fh, D))}
    _B_in = _B_setup()
    return {'A_x': _A_in['x'], 'A_w_r': _A_in['w_r'], 'A_w_i': _A_in['w_i'], 'B_x': _B_in['x'], 'B_w_in': _B_in['w_in'], 'B_w_sp': _B_in['w_sp'], 'B_b_sp': _B_in['b_sp'], 'B_w_out': _B_in['w_out']}

def reference(A_x, A_w_r, A_w_i, B_x, B_w_in, B_w_sp, B_b_sp, B_w_out):

    def _A_ref(x, w_r, w_i):
        B, S, D = x.shape
        X = (lambda x, axes=(-2, -1): torch.fft.rfft2(x.float(), dim=axes))(x, axes=(-2, -1))
        W = w_r + 1j * w_i
        X = X * W
        y = (lambda x, s, axes=(-2, -1): torch.fft.irfft2(x, s=s, dim=axes).to(torch.bfloat16))(X, (S, D), axes=(-2, -1))
        return y

    def _B_ref(x, w_in, w_sp, b_sp, w_out):
        B, S, D = x.shape
        F = w_in.shape[-1]
        Fh = F // 2
        z = _gelu(torch.einsum('bsd,df->bsf', x, w_in))
        z1 = z[..., :Fh].clone()
        z2 = _layernorm(z[..., Fh:], axis=-1)
        g = torch.einsum('ts,bsf->btf', w_sp, z2) + b_sp[None, :, None]
        return torch.einsum('bsf,fd->bsd', z1 * g, w_out)
    x = A_x
    xn = _layernorm(x, axis=-1)
    s = _A_ref(xn, A_w_r, A_w_i)
    g = _silu(_B_ref(xn, B_w_in, B_w_sp, B_b_sp, B_w_out))
    return x + s * g
```

```cpp
#include <hip/hip_runtime.h>
#include <stdint.h>

#define GAS __attribute__((address_space(1)))
#define LAS __attribute__((address_space(3)))
#define DI __device__ __forceinline__
typedef unsigned v4u __attribute__((ext_vector_type(4)));
typedef unsigned v2u __attribute__((ext_vector_type(2)));
typedef float f32x2 __attribute__((ext_vector_type(2)));
typedef float f32x4 __attribute__((ext_vector_type(4)));
typedef float f32x16 __attribute__((ext_vector_type(16)));
typedef __bf16 bf16x2 __attribute__((ext_vector_type(2)));
typedef __bf16 bf16x8 __attribute__((ext_vector_type(8)));
typedef _Float16 f16x2 __attribute__((ext_vector_type(2)));
typedef _Float16 f16x8 __attribute__((ext_vector_type(8)));
typedef short s16x4 __attribute__((ext_vector_type(4)));
typedef GAS unsigned gu32;
typedef GAS unsigned long long gu64;
typedef __amdgpu_buffer_rsrc_t rsrc_t;
#define RLX_AGENT __ATOMIC_RELAXED, __HIP_MEMORY_SCOPE_AGENT

namespace {

constexpr unsigned WS_BASE = 0;
constexpr unsigned WS_F1 = 4096;
constexpr unsigned WS_F2 = WS_F1 + 256 * 128;
constexpr unsigned WS_ST = WS_F2 + 256 * 128;
constexpr unsigned WS_Z2N = WS_ST + 224 * 1024;
constexpr unsigned WS_H = WS_Z2N + 7 * 4 * 128 * 1024;
constexpr unsigned WS_XD = WS_H + 32 * 56 * 2048;
constexpr unsigned WS_WT = WS_XD + 448 * 4096;
constexpr unsigned WS_Y = WS_WT + 228 * 8192;
constexpr unsigned WS_DBG = WS_Y + 32 * 29 * 2048;
constexpr unsigned WS_END = WS_DBG + 256 * 32 * 8;

constexpr int LDS_BYTES = 160 * 1024;
constexpr unsigned kSpinCap = 1u << 18;
constexpr int SC1 = 16;

template <int I> struct IC { static constexpr int v = I; };
template <int B, int E, class F> DI void sfor(F&& f) {
  if constexpr (B < E) { f(IC<B>{}); sfor<B + 1, E>(f); }
}
template <int N> DI void wait_vm() { asm volatile("s_waitcnt vmcnt(%0)" ::"n"(N) : "memory"); }
DI void wait_lgkm() { asm volatile("s_waitcnt lgkmcnt(0)" ::: "memory"); }
DI void wg_barrier() {
  asm volatile("s_waitcnt lgkmcnt(0)" ::: "memory");
  __builtin_amdgcn_s_barrier();
  asm volatile("" ::: "memory");
}

DI float as_f(unsigned u) { return __builtin_bit_cast(float, u); }
DI unsigned as_u(float f) { return __builtin_bit_cast(unsigned, f); }
DI unsigned pk_bf16(float a, float b) { f32x2 f = {a, b}; return __builtin_bit_cast(unsigned, __builtin_convertvector(f, bf16x2)); }
DI unsigned pk_f16(float a, float b) { f32x2 f = {a, b}; return __builtin_bit_cast(unsigned, __builtin_convertvector(f, f16x2)); }
DI float bf_lo(unsigned u) { return __builtin_bit_cast(float, u << 16); }
DI float bf_hi(unsigned u) { return __builtin_bit_cast(float, u & 0xffff0000u); }
DI float rbf(float x) { return bf_lo(pk_bf16(x, x)); }
DI float dot2(unsigned a, unsigned b, float c) {
  return __builtin_amdgcn_fdot2_f32_bf16(__builtin_bit_cast(bf16x2, a), __builtin_bit_cast(bf16x2, b), c, false);
}
DI f32x16 mfma_bf(v4u a, v4u b, f32x16 c) {
  return __builtin_amdgcn_mfma_f32_32x32x16_bf16(__builtin_bit_cast(bf16x8, a), __builtin_bit_cast(bf16x8, b), c, 0, 0, 0);
}
DI f32x16 mfma_h(v4u a, v4u b, f32x16 c) {
  return __builtin_amdgcn_mfma_f32_32x32x16_f16(__builtin_bit_cast(f16x8, a), __builtin_bit_cast(f16x8, b), c, 0, 0, 0);
}
DI float xhalf_sum(float x) {
  auto t = __builtin_amdgcn_permlane32_swap(__builtin_bit_cast(unsigned, x), __builtin_bit_cast(unsigned, x), false, false);
  return as_f(t[0]) + as_f(t[1]);
}
template <int CTRL> DI float dpp(float x) {
  return __builtin_bit_cast(float, __builtin_amdgcn_mov_dpp(__builtin_bit_cast(int, x), CTRL, 0xf, 0xf, true));
}
DI float sum8(float x) {
  x += dpp<0xB1>(x);
  x += dpp<0x4E>(x);
  x += dpp<0x141>(x);
  return x;
}
DI v2u tr_read(LAS char* p) { return __builtin_bit_cast(v2u, __builtin_amdgcn_ds_read_tr16_b64_v4i16((LAS s16x4*)p)); }
DI v4u cat(v2u a, v2u b) { v4u r = {a[0], a[1], b[0], b[1]}; return r; }
DI void glds(v4u rs, LAS char* dst, int voff, int soff) {
  unsigned keep;
  asm volatile("s_mov_b32 %0, m0\n\ts_mov_b32 m0, %1\n\ts_nop 0\n\tbuffer_load_dwordx4 %2, %3, %4 offen lds\n\ts_mov_b32 m0, %0"
               : "=&s"(keep) : "s"((unsigned)(size_t)dst), "v"(voff), "s"(rs), "s"(soff) : "memory");
}
DI v4u mk_desc(const void* p, unsigned bytes) {
  const unsigned long long a = (unsigned long long)p;
  v4u d = {(unsigned)a, (unsigned)(a >> 32) & 0xffffu, bytes, 0x00020000u};
  return d;
}
DI unsigned load_epoch(const unsigned char* ws) {
  unsigned b;
  asm volatile("s_load_dword %0, %1, 0x0\n\ts_waitcnt lgkmcnt(0)" : "=s"(b) : "s"(ws) : "memory");
  return b;
}
DI v4u ld_sc1(rsrc_t rs, int voff, int soff) { return __builtin_amdgcn_raw_buffer_load_b128(rs, voff, soff, SC1); }
DI v4u ld_frag(rsrc_t rs, int lane16, int i, int soff) { return ld_sc1(rs, lane16 + (i & 3) * 1024, soff + (i >> 2) * 4096); }
DI v2u ld64_sc1(rsrc_t rs, int voff, int soff) { return __builtin_amdgcn_raw_buffer_load_b64(rs, voff, soff, SC1); }
DI void st_sc1(v4u v, rsrc_t rs, int voff, int soff) { __builtin_amdgcn_raw_buffer_store_b128(v, rs, voff, soff, SC1); }
DI void st64_sc1(v2u v, rsrc_t rs, int voff, int soff) { __builtin_amdgcn_raw_buffer_store_b64(v, rs, voff, soff, SC1); }
DI rsrc_t mk_rsrc(const void* p, unsigned bytes) { return __builtin_amdgcn_make_buffer_rsrc((void*)p, 0, bytes, 0x00020000); }

DI void poll_flags(gu32* p, unsigned tag) {
  for (unsigned s = 0;; ++s) {
    const unsigned v = __hip_atomic_load(p, RLX_AGENT);
    if (__all(v == tag)) break;
    if (s > kSpinCap) break;
    __builtin_amdgcn_s_sleep(1);
  }
  __builtin_amdgcn_fence(__ATOMIC_ACQUIRE, "wavefront");
}

DI float gelu_t(float x) {
  const float c1 = -2.302208198f, c2 = -2.302208198f * 0.044715f;
  const float e = __builtin_amdgcn_exp2f(x * __builtin_fmaf(x * x, c2, c1));
  return x * __builtin_amdgcn_rcpf(1.0f + e);
}
DI f32x2 ln_consts(float s, float ss, float inv_n) {
  const float mean = s * inv_n;
  const float var = __builtin_fmaxf(__builtin_fmaf(-mean, mean, ss * inv_n), 0.0f);
  const float den = rbf(__builtin_sqrtf(rbf(rbf(var) + 1e-5f)));
  f32x2 r = {rbf(mean), 1.0f / den};
  return r;
}
DI float cosr(float rev) { return __builtin_amdgcn_cosf(rev); }
DI float sinr(float rev) { return __builtin_amdgcn_sinf(rev); }

#define STAMP(i)

constexpr int p1_wait(int c) {
  const int need = c == 0 ? 3 : c == 1 ? 6 : c == 2 ? 9 : c == 3 ? 13 : c == 4 ? 17 : 19 + 2 * (c - 5);
  const int m = c - 1 < 0 ? 0 : (c - 1 > 8 ? 8 : c - 1);
  return 19 + 2 * m - need;
}

__global__ __launch_bounds__(512, 2) void fused_kernel(const unsigned short* __restrict__ x, const unsigned short* __restrict__ w_r,
                                                       const unsigned short* __restrict__ w_i, const unsigned short* __restrict__ w_in,
                                                       const unsigned short* __restrict__ w_sp, const unsigned short* __restrict__ b_sp,
                                                       const unsigned short* __restrict__ w_out, unsigned short* __restrict__ out,
                                                       unsigned char* ws) {
  extern __shared__ __attribute__((aligned(16))) char smem_[];
  LAS char* const L = (LAS char*)smem_;
  const int tid = threadIdx.x, lane = tid & 63, w = __builtin_amdgcn_readfirstlane(tid >> 6);
  const int r = lane & 31, h = lane >> 5;
  const int xcd = blockIdx.x & 7, rank = blockIdx.x >> 3;
  const rsrc_t rs_ws = mk_rsrc(ws, WS_END);
  const v4u rs_x = mk_desc(x, 2048 * 448 * 2);
  gu32* const wsw = (gu32*)ws;
  STAMP(0);
  const unsigned ONES = 0x3f803f80u;
  const int tq = (lane & 15) >> 2, tp = lane & 3, tblk = (lane >> 4) & 1;

  if (rank < 28) {
    const int tm = xcd * 4 + rank / 7, tn = rank % 7;
    const int id = tm * 7 + tn;
    const int mw = w & 1, nw = w >> 1;
    constexpr int X_OFF = 0, RING = 57344, MISC = 155648;
    constexpr int XST = MISC, STP = MISC + 512, CSUM = MISC + 512 + 2048;
    constexpr int Z1_OFF = 131072;
    const v4u rs_win = mk_desc(w_in, 448 * 1792 * 2);
    const v4u rs_wsp = mk_desc(w_sp, 2048 * 2048 * 2);
    const v4u rs_wout = mk_desc(w_out, 896 * 448 * 2);
    const int rot = (tm + tn) % 7;

    const int xrow = 8 * w + (lane >> 3);
    const int x_voff = xrow * 896 + (((lane & 7) ^ ((xrow >> 1) & 7)) << 4);
    const int x_soff = tm * 64 * 896;
    int w_voff[2];
#pragma unroll
    for (int ii = 0; ii < 2; ++ii) {
      const int row = 8 * w + 4 * ii + (lane >> 4);
      w_voff[ii] = row * 3584 + (((lane & 15) ^ (((row & 3) << 2) | ((row >> 2) & 3))) << 4);
    }
    auto kb_of = [&](int i) { int k = i + rot; return k >= 7 ? k - 7 : k; };
    auto issue_x = [&](int i) { glds(rs_x, L + X_OFF + i * 8192 + w * 1024, x_voff, x_soff + kb_of(i) * 128); };
    auto issue_w = [&](int c) {
      const int i = c % 7, col = (c < 7 ? 896 : 0) + 128 * tn;
      const int so = kb_of(i) * 64 * 3584 + col * 2;
      LAS char* d = L + RING + (c % 6) * 16384 + w * 2048;
      glds(rs_win, d, w_voff[0], so);
      glds(rs_win, d + 1024, w_voff[1], so);
    };
    issue_x(0); issue_w(0); issue_x(1); issue_w(1); issue_x(2); issue_w(2);
    issue_x(3); issue_x(4); issue_w(3); issue_x(5); issue_x(6); issue_w(4); issue_w(5);
    const unsigned tag = load_epoch(ws) + 1u;

    const int frow = 32 * mw + r;
    int xa[4];
#pragma unroll
    for (int k = 0; k < 4; ++k) xa[k] = X_OFF + frow * 128 + (((2 * k + h) ^ ((frow >> 1) & 7)) << 4);
    int wa[2];
#pragma unroll
    for (int t = 0; t < 2; ++t)
      wa[t] = RING + (8 * h + 4 * t + tq) * 256 + (((4 * nw + 2 * tblk + (tp >> 1)) ^ ((tq << 2) | ((2 * h + t) & 3))) << 4) + 8 * (tp & 1);

    unsigned z2p[8], z1p[8];
    f32x16 acc;
    float cs;
    auto z_epilogue = [&](unsigned (&zp)[8]) {
      cs = xhalf_sum(cs);
      if (h == 0) *(LAS float*)(L + CSUM + w * 128 + r * 4) = cs;
      const f32x2 st = *(LAS f32x2*)(L + XST + frow * 8);
      const float mr = -st[0] * st[1];
#pragma unroll
      for (int g = 0; g < 4; ++g) {
        const f32x4 c4 = *(LAS f32x4*)(L + CSUM + w * 128 + (8 * g + 4 * h) * 4);
#pragma unroll
        for (int j = 0; j < 2; ++j) {
          const float p0 = __builtin_fmaf(acc[4 * g + 2 * j], st[1], mr * c4[2 * j]);
          const float p1 = __builtin_fmaf(acc[4 * g + 2 * j + 1], st[1], mr * c4[2 * j + 1]);
          const unsigned pb = pk_bf16(p0, p1);
          zp[2 * g + j] = pk_bf16(gelu_t(bf_lo(pb)), gelu_t(bf_hi(pb)));
        }
      }
    };

    sfor<0, 14>([&](auto C) {
      constexpr int c = C.v;
      wait_vm<p1_wait(c)>();
      wg_barrier();
      if constexpr (c >= 1 && c <= 8) issue_w(c + 5);
      if constexpr (c == 0) STAMP(1);
      if constexpr (c == 7) {
        if (w == 7) {
          float s = 0.f, ss = 0.f;
#pragma unroll
          for (int q = 0; q < 4; ++q) { const f32x2 p = *(LAS f32x2*)(L + STP + q * 512 + lane * 8); s += p[0]; ss += p[1]; }
          v2u g0 = {__builtin_bit_cast(unsigned, s), tag}, g1 = {__builtin_bit_cast(unsigned, ss), tag};
          st64_sc1(g0, rs_ws, lane * 8, WS_ST + id * 1024);
          st64_sc1(g1, rs_ws, lane * 8 + 512, WS_ST + id * 1024);
        }
      }
      if constexpr (c % 7 == 0) {
#pragma unroll
        for (int i = 0; i < 16; ++i) acc[i] = 0.f;
        cs = 0.f;
      }
#pragma unroll
      for (int k = 0; k < 4; ++k) {
        const v4u xf = *(LAS v4u*)(L + xa[k] + (c % 7) * 8192);
        const v4u wf = cat(tr_read(L + wa[0] + (c % 6) * 16384 + k * 4096), tr_read(L + wa[1] + (c % 6) * 16384 + k * 4096));
        acc = mfma_bf(wf, xf, acc);
#pragma unroll
        for (int j = 0; j < 4; ++j) cs = dot2(wf[j], ONES, cs);
      }
      if constexpr (c == 5) {
        float s = 0.f, ss = 0.f;
#pragma unroll
        for (int i = 0; i < 7; ++i) {
          const v4u v = *(LAS v4u*)(L + X_OFF + i * 8192 + w * 1024 + lane * 16);
#pragma unroll
          for (int j = 0; j < 4; ++j) { s = dot2(v[j], ONES, s); ss = dot2(v[j], v[j], ss); }
        }
        s = sum8(s); ss = sum8(ss);
        const f32x2 st = ln_consts(s, ss, 1.0f / 448.0f);
        if ((lane & 7) == 0) *(LAS f32x2*)(L + XST + xrow * 8) = st;
      }
      if constexpr (c == 6) {
        z_epilogue(z2p);
        float s = 0.f, ss = 0.f;
#pragma unroll
        for (int j = 0; j < 8; ++j) { s = dot2(z2p[j], ONES, s); ss = dot2(z2p[j], z2p[j], ss); }
        s = xhalf_sum(s); ss = xhalf_sum(ss);
        f32x2 p = {s, ss};
        if (h == 0) *(LAS f32x2*)(L + STP + nw * 512 + frow * 8) = p;
        STAMP(2);
      }
    });
    STAMP(3);
    z_epilogue(z1p);
    {
      float tot;
      for (unsigned s = 0;; ++s) {
        bool ok = true;
        tot = 0.f;
#pragma unroll
        for (int q = 0; q < 7; ++q) {
          const v2u g = ld64_sc1(rs_ws, h * 512 + frow * 8, WS_ST + (tm * 7 + q) * 1024);
          ok &= g[1] == tag;
          tot += as_f(g[0]);
        }
        if (__all(ok) || s > kSpinCap) break;
        __builtin_amdgcn_s_sleep(1);
      }
      auto t = __builtin_amdgcn_permlane32_swap(__builtin_bit_cast(unsigned, tot), __builtin_bit_cast(unsigned, tot), false, false);
      const f32x2 st = ln_consts(as_f(t[0]), as_f(t[1]), 1.0f / 896.0f);
      STAMP(4);
      wg_barrier();
      LAS char* T = L + Z1_OFF + w * 2048;
#pragma unroll
      for (int g = 0; g < 4; ++g) {
        v2u o;
#pragma unroll
        for (int j = 0; j < 2; ++j) {
          const unsigned db = pk_bf16(bf_lo(z2p[2 * g + j]) - st[0], bf_hi(z2p[2 * g + j]) - st[0]);
          o[j] = pk_bf16(bf_lo(db) * st[1], bf_hi(db) * st[1]);
        }
        *(LAS v2u*)(T + r * 64 + ((g ^ ((r >> 2) & 3)) << 4) + 8 * h) = o;
      }
#pragma unroll
      for (int u = 0; u < 2; ++u) {
        v2u lo, hi;
        {
          const int row = 16 * u + 8 * h + tq;
          lo = tr_read(T + row * 64 + (((2 * tblk + (tp >> 1)) ^ ((row >> 2) & 3)) << 4) + 8 * (tp & 1));
        }
        {
          const int row = 16 * u + 8 * h + 4 + tq;
          hi = tr_read(T + row * 64 + (((2 * tblk + (tp >> 1)) ^ ((row >> 2) & 3)) << 4) + 8 * (tp & 1));
        }
        st_sc1(cat(lo, hi), rs_ws, lane * 16, WS_Z2N + ((tn * 4 + nw) * 128 + 4 * tm + 2 * mw + u) * 1024);
      }
      wait_lgkm();
#pragma unroll
      for (int u = 0; u < 2; ++u) {
        v4u v = {z1p[4 * u], z1p[4 * u + 1], z1p[4 * u + 2], z1p[4 * u + 3]};
        *(LAS v4u*)(T + u * 1024 + lane * 16) = v;
      }
    }
    LAS char* const AS = L + w * 16384;
    auto issue_a = [&](int hf) {
#pragma unroll
      for (int i = 0; i < 16; ++i) {
        const int row = 4 * i + (lane >> 4);
        glds(rs_wsp, AS + i * 1024, (lane >> 4) * 4096 + (((lane & 15) ^ (row & 15)) << 4), (tm * 64 + 4 * i) * 4096 + w * 512 + hf * 256);
      }
    };
    v4u wsp[2][16];
    auto read_a = [&](auto HF) {
      constexpr int hf = HF.v;
#pragma unroll
      for (int ks = 0; ks < 8; ++ks)
#pragma unroll
        for (int tb = 0; tb < 2; ++tb)
          wsp[tb][hf * 8 + ks] = *(LAS v4u*)(AS + (32 * tb + r) * 256 + (((2 * ks + h) ^ (r & 15)) << 4));
    };
    issue_a(0);
    wait_vm<16>();
    wg_barrier();
    if (tid == 0) __hip_atomic_store(wsw + (WS_F1 / 4) + id * 32, tag, RLX_AGENT);
    STAMP(5);
    wait_vm<0>();
    read_a(IC<0>{});
    wait_lgkm();
    issue_a(1);
    const int e_tb = w >> 2, e_u = (w >> 1) & 1, e_slot = 32 * (w & 1) + (lane >> 1), e_a = lane & 1;
    const float bias = bf_lo(b_sp[tm * 64 + 32 * e_tb + (e_slot & 31)]);
    poll_flags(wsw + (WS_F1 / 4) + ((4 * w + (lane & 3)) * 7 + tn) * 32, tag);
    STAMP(6);
    const int q0 = tm & 3;
    v4u bw[16];
#pragma unroll
    for (int ks = 0; ks < 16; ++ks) bw[ks] = ld_frag(rs_ws, lane * 16, ks, WS_Z2N + ((tn * 4 + q0) * 128 + 16 * w) * 1024);
    __builtin_amdgcn_sched_barrier(0);
    read_a(IC<1>{});
    wait_lgkm();
    sfor<0, 4>([&](auto QI) {
      constexpr int qi = QI.v;
      const int q = (q0 + qi) & 3, qn = (q0 + qi + 1) & 3;
      f32x16 a0, a1;
#pragma unroll
      for (int i = 0; i < 16; ++i) { a0[i] = 0.f; a1[i] = 0.f; }
#pragma unroll
      for (int ks = 0; ks < 16; ++ks) {
        const v4u cur = bw[ks];
        if constexpr (qi < 3) bw[ks] = ld_frag(rs_ws, lane * 16, ks, WS_Z2N + ((tn * 4 + qn) * 128 + 16 * w) * 1024);
        a0 = mfma_bf(cur, wsp[0][ks], a0);
        a1 = mfma_bf(cur, wsp[1][ks], a1);
        __builtin_amdgcn_sched_barrier(0);
      }
      LAS char* RB = AS + (qi & 1) * 8192;
#pragma unroll
      for (int g = 0; g < 4; ++g) {
        f32x4 v0 = {a0[4 * g], a0[4 * g + 1], a0[4 * g + 2], a0[4 * g + 3]};
        f32x4 v1 = {a1[4 * g], a1[4 * g + 1], a1[4 * g + 2], a1[4 * g + 3]};
        *(LAS f32x4*)(RB + g * 1024 + ((lane ^ (8 * (g & 1))) << 4)) = v0;
        *(LAS f32x4*)(RB + (4 + g) * 1024 + ((lane ^ (8 * (g & 1))) << 4)) = v1;
      }
      wg_barrier();
      f32x4 s = {0.f, 0.f, 0.f, 0.f};
#pragma unroll
      for (int p = 0; p < 8; ++p)
        s += *(LAS f32x4*)(L + p * 16384 + (qi & 1) * 8192 + (e_tb * 4 + 2 * e_u + e_a) * 1024 + ((e_slot ^ (8 * e_a)) << 4));
      const v2u z1 = *(LAS v2u*)(L + Z1_OFF + (q * 2 + e_tb) * 2048 + e_u * 1024 + e_slot * 16 + e_a * 8);
      v2u o;
#pragma unroll
      for (int j = 0; j < 2; ++j) {
        const unsigned gb = pk_bf16(s[2 * j], s[2 * j + 1]);
        const unsigned g2 = pk_bf16(bf_lo(gb) + bias, bf_hi(gb) + bias);
        o[j] = pk_bf16(bf_lo(z1[j]) * bf_lo(g2), bf_hi(z1[j]) * bf_hi(g2));
      }
      st64_sc1(o, rs_ws, e_slot * 16 + e_a * 8, WS_H + ((tm * 56 + tn * 8 + q * 2 + e_u) * 2 + e_tb) * 1024);
    });
    STAMP(7);
    wait_vm<0>();
    wg_barrier();
    if (tid == 0) __hip_atomic_store(wsw + (WS_F2 / 4) + id * 32, tag, RLX_AGENT);
    STAMP(8);

    const int e_t = tid >> 3, e_q = tid & 7;
    const v4u xres = *(const GAS v4u*)((const GAS char*)x + (size_t)(tm * 64 + e_t) * 896 + tn * 128 + e_q * 16);
    f32x16 c4[2][2];
#pragma unroll
    for (int i = 0; i < 16; ++i) { c4[0][0][i] = 0.f; c4[0][1][i] = 0.f; c4[1][0][i] = 0.f; c4[1][1][i] = 0.f; }
    if (w < 5) {
      const int k0 = 11 * w;
      LAS char* WO = L + k0 * 2048;
      {
        const int row = lane >> 3;
        const int voff = row * 896 + (((lane & 7) ^ (((row >> 1) & 1) << 2)) << 4);
#pragma unroll
        for (int i = 0; i < 24; ++i)
          if (i < 22 || w == 4) glds(rs_wout, WO + i * 1024, voff, (k0 * 16 + 8 * i) * 896 + tn * 128);
      }
      poll_flags(wsw + (WS_F2 / 4) + (tm * 7 + (lane % 7)) * 32, tag);
      STAMP(9);
      v4u hf[12][2];
#pragma unroll
      for (int i = 0; i < 12; ++i)
#pragma unroll
        for (int tb = 0; tb < 2; ++tb)
          if (i < 11 || w == 4) hf[i][tb] = ld_frag(rs_ws, lane * 16, i * 2 + tb, WS_H + (tm * 56 + k0) * 2048);
      __builtin_amdgcn_sched_barrier(0);
#pragma unroll
      for (int i = 0; i < 12; ++i) {
        if (i < 11 || w == 4) {
#pragma unroll
          for (int db = 0; db < 2; ++db) {
            const int ch = (4 * db + 2 * tblk + (tp >> 1)) ^ (((tq >> 1) & 1) << 2);
            LAS char* p = WO + i * 2048 + (4 * h + tq) * 128 + (ch << 4) + 8 * (tp & 1);
            const v4u wf = cat(tr_read(p), tr_read(p + 1024));
            c4[db][0] = mfma_bf(wf, hf[i][0], c4[db][0]);
            c4[db][1] = mfma_bf(wf, hf[i][1], c4[db][1]);
          }
        }
      }
    } else {
      const int c0 = 10 * (w - 5);
      v4u gf[10][2];
#pragma unroll
      for (int db = 0; db < 2; ++db) {
        const float th = (float)(tn * 64 + 32 * db + r) * (1.0f / 448.0f);
#pragma unroll
        for (int i = 0; i < 10; ++i)
#pragma unroll
          for (int j = 0; j < 4; ++j) {
            const float a = (float)(8 * (c0 + i) + 4 * h + j) * th;
            gf[i][db][j] = pk_bf16(cosr(a), -sinr(a));
          }
      }
      {
        const int c = c0 + (lane % 10);
        poll_flags(wsw + (WS_F2 / 4) + (224 + (c > 28 ? 28 : c)) * 32, tag);
      }
      v4u yf[10][2];
#pragma unroll
      for (int i = 0; i < 10; ++i)
#pragma unroll
        for (int tb = 0; tb < 2; ++tb)
          if (i < 9 || w != 7) yf[i][tb] = ld_frag(rs_ws, lane * 16, i * 2 + tb, WS_Y + (tm * 29 + c0) * 2048);
      __builtin_amdgcn_sched_barrier(0);
#pragma unroll
      for (int i = 0; i < 10; ++i) {
        if (i < 9 || w != 7) {
#pragma unroll
          for (int db = 0; db < 2; ++db) {
            c4[db][0] = mfma_bf(gf[i][db], yf[i][0], c4[db][0]);
            c4[db][1] = mfma_bf(gf[i][db], yf[i][1], c4[db][1]);
          }
        }
      }
    }
    STAMP(10);
    wg_barrier();
#pragma unroll
    for (int db = 0; db < 2; ++db)
#pragma unroll
      for (int tb = 0; tb < 2; ++tb)
#pragma unroll
        for (int g = 0; g < 4; ++g) {
          f32x4 v = {c4[db][tb][4 * g], c4[db][tb][4 * g + 1], c4[db][tb][4 * g + 2], c4[db][tb][4 * g + 3]};
          *(LAS f32x4*)(L + w * 16384 + ((db * 2 + tb) * 4 + g) * 1024 + ((lane ^ ((db * 4 + g) << 1)) << 4)) = v;
        }
    wg_barrier();
    {
      f32x4 so[2], sy[2];
#pragma unroll
      for (int hh = 0; hh < 2; ++hh) {
        const int off = (((e_q >> 2) * 2 + (e_t >> 5)) * 4 + (e_q & 3)) * 1024 + ((((e_t & 31) + 32 * hh) ^ (e_q << 1)) << 4);
        so[hh] = *(LAS f32x4*)(L + off);
#pragma unroll
        for (int p = 1; p < 5; ++p) so[hh] += *(LAS f32x4*)(L + p * 16384 + off);
        sy[hh] = *(LAS f32x4*)(L + 5 * 16384 + off);
#pragma unroll
        for (int p = 6; p < 8; ++p) sy[hh] += *(LAS f32x4*)(L + p * 16384 + off);
      }
      v4u o;
#pragma unroll
      for (int j = 0; j < 4; ++j) {
        const unsigned ob = pk_bf16(so[j >> 1][2 * (j & 1)], so[j >> 1][2 * (j & 1) + 1]);
        const float o0 = bf_lo(ob), o1 = bf_hi(ob);
        const unsigned gb = pk_bf16(o0 * __builtin_amdgcn_rcpf(1.0f + __builtin_amdgcn_exp2f(-1.44269504f * o0)),
                                    o1 * __builtin_amdgcn_rcpf(1.0f + __builtin_amdgcn_exp2f(-1.44269504f * o1)));
        const unsigned sb = pk_bf16(sy[j >> 1][2 * (j & 1)], sy[j >> 1][2 * (j & 1) + 1]);
        const unsigned pb = pk_bf16(bf_lo(sb) * bf_lo(gb), bf_hi(sb) * bf_hi(gb));
        o[j] = pk_bf16(bf_lo(xres[j]) + bf_lo(pb), bf_hi(xres[j]) + bf_hi(pb));
      }
      *(GAS v4u*)((GAS char*)out + (size_t)(tm * 64 + e_t) * 896 + tn * 128 + e_q * 16) = o;
    }
    STAMP(11);
  } else {
    const int j = xcd * 4 + (rank - 28);
    constexpr int X_OFF = 0, WRAW = 57344, WSZ = 29696, TW = WRAW + 2 * WSZ, XST = TW + 32768;
    const v4u rs_wr = mk_desc(w_r, 2048 * 225 * 2);
    const v4u rs_wi = mk_desc(w_i, 2048 * 225 * 2);
    {
      const int lr = 8 * w + (lane >> 3), rho = lr & 31;
      const int m = (rho & 19) | ((rho & 4) << 1) | ((rho & 8) >> 1);
      const int voff = (2 * m + (lr >> 5)) * 896 + (((lane & 7) ^ ((lr >> 1) & 7)) << 4);
#pragma unroll
      for (int i = 0; i < 7; ++i) glds(rs_x, L + X_OFF + i * 8192 + w * 1024, voff, j * 64 * 896 + i * 128);
    }
#pragma unroll
    for (int i = 0; i < 4; ++i) {
      const int p = w * 4 + i;
      if (p < 29) {
        const int vo = (p == 28 ? (lane & 7) : lane) * 16;
        glds(rs_wr, L + WRAW + p * 1024, vo, j * 28800 + p * 1024);
        glds(rs_wi, L + WRAW + WSZ + p * 1024, vo, j * 28800 + p * 1024);
      }
    }
#pragma unroll
    for (int i = 0; i < 4; ++i) {
      const int e = w * 4 + i, which = e >> 4, blk = (e >> 3) & 1, rp = e & 7;
      f32x4 v;
#pragma unroll
      for (int q = 0; q < 2; ++q) {
        const int reg = 2 * rp + q, cr = (reg & 3) + 8 * (reg >> 2) + 4 * h;
        const int prod = which ? (2 * r + blk) * cr : (2 * cr + blk) * r;
        const float a = (float)prod * (1.0f / 2048.0f);
        v[2 * q] = 0.125f * cosr(a);
        v[2 * q + 1] = 0.125f * sinr(a);
      }
      *(LAS f32x4*)(L + TW + e * 1024 + lane * 16) = v;
    }
    const unsigned tag = load_epoch(ws) + 1u;
    wait_vm<0>();
    {
      float s = 0.f, ss = 0.f;
#pragma unroll
      for (int i = 0; i < 7; ++i) {
        const v4u v = *(LAS v4u*)(L + X_OFF + i * 8192 + w * 1024 + lane * 16);
#pragma unroll
        for (int q = 0; q < 4; ++q) { s = dot2(v[q], ONES, s); ss = dot2(v[q], v[q], ss); }
      }
      s = sum8(s); ss = sum8(ss);
      f32x2 st = ln_consts(s, ss, 1.0f / 448.0f);
      st[1] *= 0.0625f;
      st[0] = -st[0] * st[1];
      if ((lane & 7) == 0) {
        *(LAS float*)(L + XST + (8 * w + (lane >> 3)) * 4) = st[0];
        *(LAS float*)(L + XST + 256 + (8 * w + (lane >> 3)) * 4) = st[1];
      }
    }
    wg_barrier();
    STAMP(1);
    f32x16 xd[2][2];
    float cs[2] = {0.f, 0.f};
#pragma unroll
    for (int i = 0; i < 16; ++i) { xd[0][0][i] = 0.f; xd[0][1][i] = 0.f; xd[1][0][i] = 0.f; xd[1][1][i] = 0.f; }
    {
      float th[2], ph[2], c2[2];
#pragma unroll
      for (int cb = 0; cb < 2; ++cb) {
        const int col = 32 * (w + 8 * cb) + r;
        const int k = col == 1 ? 224 : (col >> 1);
        th[cb] = (float)k * (1.0f / 448.0f);
        ph[cb] = (col != 1 && (col & 1)) ? 0.25f : 0.0f;
        c2[cb] = 2.0f * cosr(th[cb]);
      }
      int xa[4];
#pragma unroll
      for (int k = 0; k < 4; ++k) xa[k] = X_OFF + r * 128 + (((2 * k + h) ^ ((r >> 1) & 7)) << 4);
      auto gen = [&](int cb, int ks) {
        const float d0 = (float)(16 * ks + 8 * h);
        float e[8];
        e[0] = cosr(__builtin_fmaf(d0, th[cb], ph[cb]));
        e[1] = cosr(__builtin_fmaf(d0 + 1.0f, th[cb], ph[cb]));
#pragma unroll
        for (int q = 2; q < 8; ++q) e[q] = __builtin_fmaf(c2[cb], e[q - 1], -e[q - 2]);
        v4u f = {pk_bf16(e[0], e[1]), pk_bf16(e[2], e[3]), pk_bf16(e[4], e[5]), pk_bf16(e[6], e[7])};
#pragma unroll
        for (int q = 0; q < 4; ++q) cs[cb] = dot2(f[q], ONES, cs[cb]);
        return f;
      };
      if (w < 6) {
#pragma unroll 4
        for (int ks = 0; ks < 28; ++ks) {
          const v4u x0 = *(LAS v4u*)(L + xa[ks & 3] + (ks >> 2) * 8192);
          const v4u x1 = *(LAS v4u*)(L + xa[ks & 3] + (ks >> 2) * 8192 + 4096);
          const v4u f0 = gen(0, ks), f1 = gen(1, ks);
          xd[0][0] = mfma_bf(x0, f0, xd[0][0]);
          xd[0][1] = mfma_bf(x1, f0, xd[0][1]);
          xd[1][0] = mfma_bf(x0, f1, xd[1][0]);
          xd[1][1] = mfma_bf(x1, f1, xd[1][1]);
        }
      } else {
#pragma unroll 4
        for (int ks = 0; ks < 28; ++ks) {
          const v4u x0 = *(LAS v4u*)(L + xa[ks & 3] + (ks >> 2) * 8192);
          const v4u x1 = *(LAS v4u*)(L + xa[ks & 3] + (ks >> 2) * 8192 + 4096);
          const v4u f0 = gen(0, ks);
          xd[0][0] = mfma_bf(x0, f0, xd[0][0]);
          xd[0][1] = mfma_bf(x1, f0, xd[0][1]);
        }
        const int ksub = lane >> 4, kp = lane & 15;
#pragma unroll 2
        for (int g = w - 6; g < 57; g += 2) {
          const int k = 4 * g + ksub;
          v4u o;
#pragma unroll
          for (int q = 0; q < 4; ++q) {
            const int row = 2 * kp + (q >> 1) + 32 * (q & 1);
            const unsigned re = *(LAS unsigned short*)(L + WRAW + row * 450 + k * 2);
            const unsigned im = *(LAS unsigned short*)(L + WRAW + WSZ + row * 450 + k * 2);
            o[q] = re | (im << 16);
          }
          st_sc1(o, rs_ws, ksub * 8192 + kp * 16, WS_WT + g * 32768 + j * 256);
        }
      }
    }
    STAMP(2);
    wg_barrier();
#pragma unroll
    for (int cb = 0; cb < 2; ++cb) {
      if (cb == 0 || w < 6) {
        const float csum = xhalf_sum(cs[cb]);
        LAS char* T = L + (w + 8 * cb) * 4096;
#pragma unroll
        for (int rb = 0; rb < 2; ++rb)
#pragma unroll
          for (int u = 0; u < 2; ++u) {
            v4u o;
#pragma unroll
            for (int a = 0; a < 2; ++a) {
              const f32x4 mr = *(LAS f32x4*)(L + XST + (32 * rb + 16 * u + 8 * a + 4 * h) * 4);
              const f32x4 ri = *(LAS f32x4*)(L + XST + 256 + (32 * rb + 16 * u + 8 * a + 4 * h) * 4);
#pragma unroll
              for (int q = 0; q < 2; ++q)
                o[2 * a + q] = pk_f16(__builtin_fmaf(xd[cb][rb][8 * u + 4 * a + 2 * q], ri[2 * q], mr[2 * q] * csum),
                                      __builtin_fmaf(xd[cb][rb][8 * u + 4 * a + 2 * q + 1], ri[2 * q + 1], mr[2 * q + 1] * csum));
            }
            *(LAS v4u*)(T + r * 128 + (((4 * rb + 2 * u + h) ^ (r & 7)) << 4)) = o;
          }
#pragma unroll
        for (int i = 0; i < 4; ++i) {
          const int col = 8 * i + (lane >> 3);
          const v4u v = *(LAS v4u*)(T + col * 128 + (((lane & 7) ^ (col & 7)) << 4));
          st_sc1(v, rs_ws, (lane >> 3) * 4096 + (lane & 7) * 16, WS_XD + (32 * (w + 8 * cb) + 8 * i) * 4096 + j * 128);
        }
      }
    }
    wait_vm<0>();
    wg_barrier();
    if (tid == 0) __hip_atomic_store(wsw + (WS_F1 / 4) + (224 + j) * 32, tag, RLX_AGENT);
    STAMP(3);

    if (j < 29) {
      const int k = 8 * j + w;
      LAS char* const IO = L + w * 8192;
      unsigned yo[2][16];
      if (k <= 224) {
        v4u FC[2], FS[2], FN[2], AC[2], AS_[2], AN[2], BC[2], BS[2], BN[2];
#pragma unroll
        for (int s = 0; s < 2; ++s)
#pragma unroll
          for (int q = 0; q < 4; ++q) {
            const int k0 = 16 * s + 8 * (q >> 1) + 4 * h + 2 * (q & 1), k1 = k0 + 1;
            const float f0 = (float)(r * k0) * (1.0f / 32.0f), f1 = (float)(r * k1) * (1.0f / 32.0f);
            const float a0 = (float)(r * (2 * k0 + 1)) * (1.0f / 64.0f), a1 = (float)(r * (2 * k1 + 1)) * (1.0f / 64.0f);
            const float b0 = (float)(k0 * (2 * r + 1)) * (1.0f / 64.0f), b1 = (float)(k1 * (2 * r + 1)) * (1.0f / 64.0f);
            FC[s][q] = pk_f16(cosr(f0), cosr(f1)); FS[s][q] = pk_f16(sinr(f0), sinr(f1)); FN[s][q] = FS[s][q] ^ 0x80008000u;
            AC[s][q] = pk_f16(cosr(a0), cosr(a1)); AS_[s][q] = pk_f16(sinr(a0), sinr(a1)); AN[s][q] = AS_[s][q] ^ 0x80008000u;
            BC[s][q] = pk_f16(cosr(b0), cosr(b1)); BS[s][q] = pk_f16(sinr(b0), sinr(b1)); BN[s][q] = BS[s][q] ^ 0x80008000u;
          }
        poll_flags(wsw + (WS_F1 / 4) + (224 + r) * 32, tag);
        STAMP(4);
        const bool realcol = (k == 0 || k == 224);
        const int pre = k == 224 ? 1 : 2 * k;
        v4u in[8];
#pragma unroll
        for (int i = 0; i < 4; ++i) in[i] = ld_sc1(rs_ws, lane * 16 + i * 1024, WS_XD + pre * 4096);
#pragma unroll
        for (int i = 0; i < 4; ++i) {
          v4u z = {0u, 0u, 0u, 0u};
          in[4 + i] = realcol ? z : ld_sc1(rs_ws, lane * 16 + i * 1024, WS_XD + (pre + 1) * 4096);
        }
        v2u wv[2][8];
#pragma unroll
        for (int ab = 0; ab < 2; ++ab)
#pragma unroll
          for (int rp = 0; rp < 8; ++rp)
            wv[ab][rp] = ld64_sc1(rs_ws, r * 8 + h * 512 + ((rp & 1) + 4 * (rp >> 1)) * 256, WS_WT + k * 8192 + ab * 4096);
#pragma unroll
        for (int i = 0; i < 8; ++i) {
          const int row = 8 * (i & 3) + (lane >> 3);
          *(LAS v4u*)(IO + (i >> 2) * 4096 + row * 128 + (((lane & 7) ^ (((row >> 1) & 1) << 2)) << 4)) = in[i];
        }
        f32x16 re[2], im[2];
        v4u pr[2][2], pi[2][2];
        auto zero = [&]() {
#pragma unroll
          for (int i = 0; i < 16; ++i) { re[0][i] = 0.f; re[1][i] = 0.f; im[0][i] = 0.f; im[1][i] = 0.f; }
        };
        auto pack = [&]() {
#pragma unroll
          for (int b = 0; b < 2; ++b)
#pragma unroll
            for (int s = 0; s < 2; ++s)
#pragma unroll
              for (int q = 0; q < 4; ++q) {
                pr[b][s][q] = pk_f16(re[b][8 * s + 2 * q], re[b][8 * s + 2 * q + 1]);
                pi[b][s][q] = pk_f16(im[b][8 * s + 2 * q], im[b][8 * s + 2 * q + 1]);
              }
        };
#pragma unroll
        for (int b = 0; b < 2; ++b)
#pragma unroll
          for (int s = 0; s < 2; ++s) {
            const int ch = (4 * b + 2 * tblk + (tp >> 1)) ^ (((tq >> 1) & 1) << 2);
            LAS char* p = IO + (16 * s + 4 * h + tq) * 128 + (ch << 4) + 8 * (tp & 1);
            pr[b][s] = cat(tr_read(p), tr_read(p + 1024));
            pi[b][s] = cat(tr_read(p + 4096), tr_read(p + 4096 + 1024));
          }
        zero();
#pragma unroll
        for (int b = 0; b < 2; ++b)
#pragma unroll
          for (int s = 0; s < 2; ++s) {
            re[b] = mfma_h(pr[b][s], FC[s], re[b]);
            im[b] = mfma_h(pi[b][s], FC[s], im[b]);
            re[b] = mfma_h(pi[b][s], FS[s], re[b]);
            im[b] = mfma_h(pr[b][s], FN[s], im[b]);
          }
#pragma unroll
        for (int b = 0; b < 2; ++b)
#pragma unroll
          for (int rp = 0; rp < 8; ++rp) {
            const f32x4 t = *(LAS f32x4*)(L + TW + (b * 8 + rp) * 1024 + lane * 16);
#pragma unroll
            for (int q = 0; q < 2; ++q) {
              const float a = re[b][2 * rp + q], c = im[b][2 * rp + q];
              re[b][2 * rp + q] = __builtin_fmaf(a, t[2 * q], c * t[2 * q + 1]);
              im[b][2 * rp + q] = __builtin_fmaf(c, t[2 * q], -a * t[2 * q + 1]);
            }
          }
        pack();
        zero();
#pragma unroll
        for (int s = 0; s < 2; ++s) {
          re[0] = mfma_h(FC[s], pr[0][s], re[0]);
          im[0] = mfma_h(FC[s], pi[0][s], im[0]);
          re[1] = mfma_h(AC[s], pr[1][s], re[1]);
          im[1] = mfma_h(AC[s], pi[1][s], im[1]);
          re[0] = mfma_h(FS[s], pi[0][s], re[0]);
          im[0] = mfma_h(FN[s], pr[0][s], im[0]);
          re[1] = mfma_h(AS_[s], pi[1][s], re[1]);
          im[1] = mfma_h(AN[s], pr[1][s], im[1]);
        }
        unsigned mx = 0;
#pragma unroll
        for (int ab = 0; ab < 2; ++ab)
#pragma unroll
          for (int rp = 0; rp < 8; ++rp)
#pragma unroll
            for (int q = 0; q < 2; ++q) {
              const unsigned u = wv[ab][rp][q];
              mx = max(mx, max(u & 0x7fff0000u, (u << 16) & 0x7fff0000u));
            }
#pragma unroll
        for (int o = 1; o < 64; o <<= 1) mx = max(mx, (unsigned)__shfl_xor((int)mx, o));
        int ex = (int)(mx >> 23);
        ex = ex < 16 ? 16 : (ex > 240 ? 240 : ex);
        const float wsc = __builtin_bit_cast(float, (unsigned)(127 + 126 - 3 - ex) << 23);
#pragma unroll
        for (int i = 0; i < 16; ++i) {
          const unsigned ua = wv[0][i >> 1][i & 1], ub = wv[1][i >> 1][i & 1];
          const float war = bf_lo(ua) * wsc, wai = bf_hi(ua) * wsc, wbr = bf_lo(ub) * wsc, wbi = bf_hi(ub) * wsc;
          const float ar = re[0][i] + re[1][i], ai = im[0][i] + im[1][i];
          const float br = re[0][i] - re[1][i], bi = im[0][i] - im[1][i];
          const float yar = __builtin_fmaf(ar, war, -ai * wai), yai = __builtin_fmaf(ar, wai, ai * war);
          const float ybr = __builtin_fmaf(br, wbr, -bi * wbi), ybi = __builtin_fmaf(br, wbi, bi * wbr);
          re[0][i] = yar + ybr; im[0][i] = yai + ybi;
          re[1][i] = yar - ybr; im[1][i] = yai - ybi;
        }
        pack();
        zero();
#pragma unroll
        for (int s = 0; s < 2; ++s) {
          re[0] = mfma_h(pr[0][s], FC[s], re[0]);
          im[0] = mfma_h(pi[0][s], FC[s], im[0]);
          re[1] = mfma_h(pr[1][s], BC[s], re[1]);
          im[1] = mfma_h(pi[1][s], BC[s], im[1]);
          re[0] = mfma_h(pi[0][s], FN[s], re[0]);
          im[0] = mfma_h(pr[0][s], FS[s], im[0]);
          re[1] = mfma_h(pi[1][s], BN[s], re[1]);
          im[1] = mfma_h(pr[1][s], BS[s], im[1]);
        }
#pragma unroll
        for (int b = 0; b < 2; ++b)
#pragma unroll
          for (int rp = 0; rp < 8; ++rp) {
            const f32x4 t = *(LAS f32x4*)(L + TW + 16384 + (b * 8 + rp) * 1024 + lane * 16);
#pragma unroll
            for (int q = 0; q < 2; ++q) {
              const float a = re[b][2 * rp + q], c = im[b][2 * rp + q];
              re[b][2 * rp + q] = __builtin_fmaf(a, t[2 * q], -c * t[2 * q + 1]);
              im[b][2 * rp + q] = __builtin_fmaf(c, t[2 * q], a * t[2 * q + 1]);
            }
          }
        pack();
        zero();
#pragma unroll
        for (int b = 0; b < 2; ++b)
#pragma unroll
          for (int s = 0; s < 2; ++s) {
            re[b] = mfma_h(FC[s], pr[b][s], re[b]);
            im[b] = mfma_h(FC[s], pi[b][s], im[b]);
            re[b] = mfma_h(FN[s], pi[b][s], re[b]);
            im[b] = mfma_h(FS[s], pr[b][s], im[b]);
          }
        const float osc = __builtin_bit_cast(float, (unsigned)(127 - 126 + 3 + ex) << 23) * ((realcol ? 1024.0f : 2048.0f) / (2048.0f * 448.0f));
        const float isc = realcol ? 0.0f : osc;
#pragma unroll
        for (int b = 0; b < 2; ++b)
#pragma unroll
          for (int i = 0; i < 16; ++i) yo[b][i] = pk_bf16(re[b][i] * osc, im[b][i] * isc);
      } else {
#pragma unroll
        for (int b = 0; b < 2; ++b)
#pragma unroll
          for (int i = 0; i < 16; ++i) yo[b][i] = 0u;
      }
      STAMP(5);
#pragma unroll
      for (int i = 0; i < 16; ++i) {
        v2u v = {yo[0][i], yo[1][i]};
        *(LAS v2u*)(IO + ((i & 3) + 8 * (i >> 2) + 4 * h) * 256 + r * 8) = v;
      }
      wg_barrier();
#pragma unroll
      for (int i = 0; i < 8; ++i) {
        const int fr = w * 8 + i;
        v4u v;
#pragma unroll
        for (int q = 0; q < 4; ++q) v[q] = *(LAS unsigned*)(L + (4 * h + q) * 8192 + fr * 128 + r * 4);
        st_sc1(v, rs_ws, lane * 16, WS_Y + (((fr >> 1) * 29 + j) * 2 + (fr & 1)) * 1024);
      }
      wait_vm<0>();
      wg_barrier();
      if (tid == 0) __hip_atomic_store(wsw + (WS_F2 / 4) + (224 + j) * 32, tag, RLX_AGENT);
      STAMP(6);
    } else if (j == 31 && w < 4) {
      poll_flags(wsw + (WS_F1 / 4) + tid * 32, tag);
      *(LAS unsigned*)(L + w * 4) = 1u;
    }
    if (j == 31) {
      wg_barrier();
      if (tid == 0) __hip_atomic_store(wsw + WS_BASE / 4, tag, RLX_AGENT);
    }
  }
}

}

extern "C" void kernel_launch(void* const* d_in, const int* in_sizes, int n_in, void* d_out, int out_size, void* d_ws, size_t ws_size,
                              hipStream_t stream) {
  (void)in_sizes; (void)n_in; (void)out_size; (void)ws_size;
  (void)hipFuncSetAttribute((const void*)fused_kernel, hipFuncAttributeMaxDynamicSharedMemorySize, LDS_BYTES);
  fused_kernel<<<256, 512, LDS_BYTES, stream>>>((const unsigned short*)d_in[0], (const unsigned short*)d_in[1], (const unsigned short*)d_in[2],
                                                (const unsigned short*)d_in[4], (const unsigned short*)d_in[5], (const unsigned short*)d_in[6],
                                                (const unsigned short*)d_in[7], (unsigned short*)d_out, (unsigned char*)d_ws);
}
```

```cpp
#include <hip/hip_runtime.h>
#include <stdint.h>

#define GAS __attribute__((address_space(1)))
#define LAS __attribute__((address_space(3)))
#define DI __device__ __forceinline__
typedef unsigned v4u __attribute__((ext_vector_type(4)));
typedef unsigned v2u __attribute__((ext_vector_type(2)));
typedef float f32x2 __attribute__((ext_vector_type(2)));
typedef float f32x4 __attribute__((ext_vector_type(4)));
typedef float f32x16 __attribute__((ext_vector_type(16)));
typedef __bf16 bf16x2 __attribute__((ext_vector_type(2)));
typedef __bf16 bf16x8 __attribute__((ext_vector_type(8)));
typedef _Float16 f16x2 __attribute__((ext_vector_type(2)));
typedef _Float16 f16x8 __attribute__((ext_vector_type(8)));
typedef short s16x4 __attribute__((ext_vector_type(4)));
typedef GAS unsigned gu32;
typedef __amdgpu_buffer_rsrc_t rsrc_t;
#define RLX_AGENT __ATOMIC_RELAXED, __HIP_MEMORY_SCOPE_AGENT

namespace {

constexpr unsigned WS_BASE = 0;
constexpr unsigned WS_F1 = 4096;
constexpr unsigned WS_F2 = WS_F1 + 256 * 128;
constexpr unsigned WS_F3 = WS_F2 + 256 * 128;
constexpr unsigned WS_ST = WS_F3 + 256 * 128;
constexpr unsigned WS_Z2N = WS_ST + 224 * 1024;
constexpr unsigned ZW = 16384 + 1024, ZQ = 8 * ZW, ZT = 4 * ZQ + 512;
constexpr unsigned WS_H = WS_Z2N + 7 * ZT + 512;
constexpr unsigned WS_XD = WS_H + 32 * 56 * 2048;
constexpr unsigned XDS = 4096 + 128, WTS = 8192 + 256;
constexpr unsigned WS_WT = WS_XD + 448 * XDS;
constexpr unsigned WS_Y = WS_WT + 240 * WTS;
constexpr unsigned WS_WO = WS_Y + 32 * 29 * 2048;
constexpr unsigned WS_END = WS_WO + 7 * 896 * 128;

constexpr int LDS_BYTES = 160 * 1024;
constexpr unsigned kSpinCap = 1u << 18;
constexpr int SC1 = 16;

template <int I> struct IC { static constexpr int v = I; };
template <int B, int E, class F> DI void sfor(F&& f) {
  if constexpr (B < E) { f(IC<B>{}); sfor<B + 1, E>(f); }
}
template <int N> DI void wait_vm() { asm volatile("s_waitcnt vmcnt(%0)" ::"n"(N) : "memory"); }
DI void wait_lgkm() { asm volatile("s_waitcnt lgkmcnt(0)" ::: "memory"); }
DI void wg_barrier() {
  asm volatile("s_waitcnt lgkmcnt(0)" ::: "memory");
  __builtin_amdgcn_s_barrier();
  asm volatile("" ::: "memory");
}
#define SCHED_FENCE() __builtin_amdgcn_sched_barrier(0)
DI void pin(v4u& v) { asm volatile("" : "+v"(v)); }
DI void pin(v2u& v) { asm volatile("" : "+v"(v)); }

DI float as_f(unsigned u) { return __builtin_bit_cast(float, u); }
DI unsigned as_u(float f) { return __builtin_bit_cast(unsigned, f); }
DI unsigned pk_bf16(f32x2 f) { return __builtin_bit_cast(unsigned, __builtin_convertvector(f, bf16x2)); }
DI unsigned pk_f16(f32x2 f) { return __builtin_bit_cast(unsigned, __builtin_convertvector(f, f16x2)); }
DI unsigned pk_bf16(float a, float b) { f32x2 f = {a, b}; return pk_bf16(f); }
DI unsigned pk_f16(float a, float b) { f32x2 f = {a, b}; return pk_f16(f); }
DI float bf_lo(unsigned u) { return as_f(u << 16); }
DI float bf_hi(unsigned u) { return as_f(u & 0xffff0000u); }
DI f32x2 unpk(unsigned u) { f32x2 r = {bf_lo(u), bf_hi(u)}; return r; }
DI float rbf(float x) { return bf_hi(pk_bf16(x, x)); }
DI float opq(float x) { asm("" : "+v"(x)); return x; }
DI f16x2 as_h2(unsigned u) { return __builtin_bit_cast(f16x2, u); }
DI unsigned as_u(f16x2 v) { return __builtin_bit_cast(unsigned, v); }
DI f16x2 hfma(f16x2 a, f16x2 b, f16x2 c) { return __builtin_elementwise_fma(a, b, c); }
DI f32x2 splat(float x) { f32x2 r = {x, x}; return r; }
DI f32x2 fma2(f32x2 a, f32x2 b, f32x2 c) { return __builtin_elementwise_fma(a, b, c); }
template <int I> DI f32x2 pair(const f32x16& v) { f32x2 r = {v[2 * I], v[2 * I + 1]}; return r; }
DI float dot2(unsigned a, unsigned b, float c) {
  return __builtin_amdgcn_fdot2_f32_bf16(__builtin_bit_cast(bf16x2, a), __builtin_bit_cast(bf16x2, b), c, false);
}
DI v4u add8(v4u a, float c) {
  float t0, t1, t2, t3, t4, t5, t6, t7;
  unsigned r0, r1, r2, r3;
  asm("v_dot2_f32_bf16 %4, %12, %16, %18\n\tv_dot2_f32_bf16 %5, %12, %17, %18\n\t"
      "v_dot2_f32_bf16 %6, %13, %16, %18\n\tv_dot2_f32_bf16 %7, %13, %17, %18\n\t"
      "v_dot2_f32_bf16 %8, %14, %16, %18\n\tv_dot2_f32_bf16 %9, %14, %17, %18\n\t"
      "v_dot2_f32_bf16 %10, %15, %16, %18\n\tv_dot2_f32_bf16 %11, %15, %17, %18\n\t"
      "v_cvt_pk_bf16_f32 %0, %4, %5\n\tv_cvt_pk_bf16_f32 %1, %6, %7\n\tv_cvt_pk_bf16_f32 %2, %8, %9\n\tv_cvt_pk_bf16_f32 %3, %10, %11"
      : "=&v"(r0), "=&v"(r1), "=&v"(r2), "=&v"(r3), "=&v"(t0), "=&v"(t1), "=&v"(t2), "=&v"(t3), "=&v"(t4), "=&v"(t5), "=&v"(t6), "=&v"(t7)
      : "v"(a[0]), "v"(a[1]), "v"(a[2]), "v"(a[3]), "s"(0x00003f80u), "s"(0x3f800000u), "v"(c));
  v4u r = {r0, r1, r2, r3};
  return r;
}
DI f32x16 mfma_bf(v4u a, v4u b, f32x16 c) {
  return __builtin_amdgcn_mfma_f32_32x32x16_bf16(__builtin_bit_cast(bf16x8, a), __builtin_bit_cast(bf16x8, b), c, 0, 0, 0);
}
DI f32x16 mfma_h(v4u a, v4u b, f32x16 c) {
  return __builtin_amdgcn_mfma_f32_32x32x16_f16(__builtin_bit_cast(f16x8, a), __builtin_bit_cast(f16x8, b), c, 0, 0, 0);
}
DI f32x16 zero16() {
  f32x16 z;
#pragma unroll
  for (int i = 0; i < 16; ++i) z[i] = 0.f;
  return z;
}
DI float xhalf_sum(float x) {
  auto t = __builtin_amdgcn_permlane32_swap(as_u(x), as_u(x), false, false);
  return as_f(t[0]) + as_f(t[1]);
}
template <int CTRL> DI float dpp(float x) { return as_f(__builtin_amdgcn_mov_dpp(as_u(x), CTRL, 0xf, 0xf, true)); }
DI float sum8(float x) {
  x += dpp<0xB1>(x);
  x += dpp<0x4E>(x);
  x += dpp<0x141>(x);
  return x;
}
DI unsigned wave_max(unsigned x) {
  x = max(x, (unsigned)__builtin_amdgcn_mov_dpp(x, 0xB1, 0xf, 0xf, true));
  x = max(x, (unsigned)__builtin_amdgcn_mov_dpp(x, 0x4E, 0xf, 0xf, true));
  x = max(x, (unsigned)__builtin_amdgcn_mov_dpp(x, 0x141, 0xf, 0xf, true));
  x = max(x, (unsigned)__builtin_amdgcn_mov_dpp(x, 0x140, 0xf, 0xf, true));
  auto a = __builtin_amdgcn_permlane16_swap(x, x, false, false);
  const unsigned a0 = a[0], a1 = a[1];
  x = max(a0, a1);
  auto b = __builtin_amdgcn_permlane32_swap(x, x, false, false);
  const unsigned b0 = b[0], b1 = b[1];
  return max(b0, b1);
}
DI v2u tr_read(LAS char* p) { return __builtin_bit_cast(v2u, __builtin_amdgcn_ds_read_tr16_b64_v4i16((LAS s16x4*)p)); }
DI v4u cat(v2u a, v2u b) { v4u r = {a[0], a[1], b[0], b[1]}; return r; }
DI void glds(v4u rs, LAS char* dst, int voff, int soff) {
  unsigned keep;
  asm volatile("s_mov_b32 %0, m0\n\ts_mov_b32 m0, %1\n\ts_nop 0\n\tbuffer_load_dwordx4 %2, %3, %4 offen lds\n\ts_mov_b32 m0, %0"
               : "=&s"(keep) : "s"((unsigned)(size_t)dst), "v"(voff), "s"(rs), "s"(soff) : "memory");
}
template <int IMM> DI v4u ldg(v4u rs, int voff, int soff) {
  v4u r;
  asm volatile("buffer_load_dwordx4 %0, %1, %2, %3 offen offset:%4" : "=v"(r) : "v"(voff), "s"(rs), "s"(soff), "n"(IMM) : "memory");
  return r;
}
DI void touch(v4u rs, LAS char* dst, int voff, int soff) {
  unsigned keep;
  asm volatile("s_mov_b32 %0, m0\n\ts_mov_b32 m0, %1\n\ts_nop 0\n\tbuffer_load_dword %2, %3, %4 offen lds\n\ts_mov_b32 m0, %0"
               : "=&s"(keep) : "s"((unsigned)(size_t)dst), "v"(voff), "s"(rs), "s"(soff) : "memory");
}
DI float max3_abs(float m, float a, float b) {
  float r;
  asm("v_max3_f32 %0, %1, |%2|, |%3|" : "=v"(r) : "v"(m), "v"(a), "v"(b));
  return r;
}
DI v2u ldg64_sc1(v4u rs, int voff, int soff) {
  v2u r;
  asm volatile("buffer_load_dwordx2 %0, %1, %2, %3 offen sc1" : "=v"(r) : "v"(voff), "s"(rs), "s"(soff) : "memory");
  return r;
}
DI v4u mk_desc(const void* p, unsigned bytes) {
  const unsigned long long a = (unsigned long long)p;
  v4u d = {(unsigned)a, (unsigned)(a >> 32) & 0xffffu, bytes, 0x00020000u};
  return d;
}
DI unsigned load_tag(const unsigned char* ws) {
  unsigned b;
  asm volatile("s_load_dword %0, %1, 0x0\n\ts_waitcnt lgkmcnt(0)" : "=s"(b) : "s"(ws) : "memory");
  return b * 0x9E3779B1u + 0x7F4A7C15u;
}
DI v4u ld_sc1(rsrc_t rs, int voff, int soff) { return __builtin_amdgcn_raw_buffer_load_b128(rs, voff, soff, SC1); }
DI v4u ld_frag(rsrc_t rs, int lane16, int i, int soff) { return ld_sc1(rs, lane16 + (i & 3) * 1024, soff + (i >> 2) * 4096); }
DI v2u ld64_sc1(rsrc_t rs, int voff, int soff) { return __builtin_amdgcn_raw_buffer_load_b64(rs, voff, soff, SC1); }
DI void st_sc1(v4u v, rsrc_t rs, int voff, int soff) {
  __builtin_amdgcn_raw_buffer_store_b128(v, rs, voff, soff, SC1);
  asm volatile("s_nop 1" ::"v"(v) : "memory");
}
DI void st64_sc1(v2u v, rsrc_t rs, int voff, int soff) { __builtin_amdgcn_raw_buffer_store_b64(v, rs, voff, soff, SC1); }
DI rsrc_t mk_rsrc(const void* p, unsigned bytes) { return __builtin_amdgcn_make_buffer_rsrc((void*)p, 0, bytes, 0x00020000); }

DI void poll_flags(gu32* p, unsigned tag) {
  for (unsigned s = 0;; ++s) {
    const unsigned v = __hip_atomic_load(p, RLX_AGENT);
    if (__all(v == tag)) break;
    if (s > kSpinCap) break;
    __builtin_amdgcn_s_sleep(1);
  }
  __builtin_amdgcn_fence(__ATOMIC_ACQUIRE, "wavefront");
}

DI void lds_arrive(LAS char* cnt, int lane) {
  wait_lgkm();
  if (lane == 0) __hip_atomic_fetch_add((LAS unsigned*)cnt, 1u, __ATOMIC_RELAXED, __HIP_MEMORY_SCOPE_WORKGROUP);
}
DI void lds_wait(LAS char* cnt, unsigned n) {
  for (unsigned s = 0; s < (kSpinCap << 4); ++s)
    if (__builtin_amdgcn_readfirstlane(__hip_atomic_load((LAS unsigned*)cnt, __ATOMIC_RELAXED, __HIP_MEMORY_SCOPE_WORKGROUP)) >= n) break;
  asm volatile("" ::: "memory");
}

DI f32x2 sigm_mul(f32x2 x, f32x2 a) {
  f32x2 e = {__builtin_amdgcn_exp2f(a[0]), __builtin_amdgcn_exp2f(a[1])};
  e += splat(1.0f);
  f32x2 r = {__builtin_amdgcn_rcpf(e[0]), __builtin_amdgcn_rcpf(e[1])};
  return x * r;
}
DI f32x2 gelu2(f32x2 x) {
  return sigm_mul(x, x * fma2(x * x, splat(-2.302208198f * 0.044715f), splat(-2.302208198f)));
}
DI f32x2 ln_consts(float s, float ss, float inv_n) {
  const float mean = s * inv_n;
  const float var = __builtin_fmaxf(__builtin_fmaf(-mean, mean, ss * inv_n), 0.0f);
  const float den = rbf(__builtin_amdgcn_sqrtf(rbf(rbf(var) + 1e-5f)));
  f32x2 r = {rbf(mean), __builtin_amdgcn_rcpf(den)};
  return r;
}
DI float cosr(float rev) { return __builtin_amdgcn_cosf(rev); }
DI float sinr(float rev) { return __builtin_amdgcn_sinf(rev); }

constexpr int P1_PRE = 2;
constexpr int IT_SG = 9, IT_PUB = 10, IT_FLAG = 13;
constexpr int p1_raw(int c) { return c == 6 ? 8 : c == 11 ? 2 : c == 12 ? 3 : c == 13 ? 3 : 0; }
constexpr int raw_base(int c) { int n = 0; for (int i = 0; i < c; ++i) n += p1_raw(i); return n; }
constexpr int p1_extra(int c) { return (c == 6 ? 1 : 0) + (c >= 8 && c <= 11 ? 2 : 0) + (c == IT_SG ? 7 : 0) + (c == IT_PUB ? 2 : 0) + p1_raw(c); }
constexpr int p1_wait(int c) {
  const int n = c + 1;
  int y = n <= 5 ? 2 * (5 - n) + P1_PRE : p1_extra(n - 6);
  for (int i = (n <= 5 ? 0 : n - 5); i < c; ++i) y += (i <= 7 ? 2 : 0) + p1_extra(i);
  if (c == IT_FLAG) {
    y = 0;
    for (int i = IT_PUB + 1; i < c; ++i) y += p1_extra(i);
  }
  return y;
}
constexpr int WO0 = 54;
constexpr int p2_early_wait(int win) {
  int y = 0;
  for (int s = 47; s <= 56; ++s) y += (s + win < 64 ? 1 : 0) + (s >= WO0 ? 1 : 0);
  return y;
}

__global__ __launch_bounds__(512, 2) void fused_kernel(const unsigned short* __restrict__ x, const unsigned short* __restrict__ w_r,
                                                       const unsigned short* __restrict__ w_i, const unsigned short* __restrict__ w_in,
                                                       const unsigned short* __restrict__ w_sp, const unsigned short* __restrict__ b_sp,
                                                       const unsigned short* __restrict__ w_out, unsigned short* __restrict__ out,
                                                       unsigned char* ws) {
  extern __shared__ __attribute__((aligned(16))) char smem_[];
  LAS char* const L = (LAS char*)smem_;
  asm volatile("" ::"s"(x), "s"(w_r), "s"(w_i), "s"(w_in), "s"(w_sp), "s"(b_sp), "s"(w_out), "s"(out), "s"(ws));
  const int tid = threadIdx.x, lane = tid & 63, w = __builtin_amdgcn_readfirstlane(tid >> 6);
  const int r = lane & 31, h = lane >> 5;
  const int xcd = blockIdx.x & 7, rank = blockIdx.x >> 3;
  const rsrc_t rs_ws = mk_rsrc(ws, WS_END);
  const v4u rs_x = mk_desc(x, 2048 * 448 * 2);
  gu32* const wsw = (gu32*)ws;
  const unsigned ONES = 0x3f803f80u;
  const int tq = (lane & 15) >> 2, tp = lane & 3, tblk = (lane >> 4) & 1;

  if (__builtin_expect(rank < 28, 1)) {
    const int tm = (xcd >> 1) * 8 + (rank < 24 ? (rank & 7) : (xcd & 1) * 4 + (rank - 24));
    const int tn = rank < 24 ? (xcd & 1) * 4 + (rank >> 3) : 3;
    const int id = tm * 7 + tn;
    const int mw = w & 1, nw = w >> 1;
    constexpr int X_OFF = 0, RING = 57344, STP = 155648, XST = STP + 2048, JUNK = XST + 256, CNT = JUNK + 256;
    constexpr int Z1_OFF = RING, PART7 = RING + 16384, A3_OFF = RING + 32768;
    const v4u rs_win = mk_desc(w_in, 448 * 1792 * 2);
    const v4u rs_wsp = mk_desc(w_sp, 2048 * 2048 * 2);
    const v4u rs_wout = mk_desc(w_out, 896 * 448 * 2);
    const v4u rs_wsd = mk_desc(ws, WS_END);
    const int rot = (tm + tn) % 7;

    const int xrow = 8 * w + (lane >> 3);
    v4u xr[7];
    sfor<0, 7>([&](auto I) { xr[I.v] = ldg<I.v * 128>(rs_x, xrow * 896 + (lane & 7) * 16, tm * 64 * 896); });
    int w_voff[2];
#pragma unroll
    for (int ii = 0; ii < 2; ++ii) {
      const int row = 8 * w + 4 * ii + (lane >> 4);
      w_voff[ii] = row * 3584 + (((lane & 15) ^ (((row & 3) << 2) | ((row >> 2) & 3))) << 4);
    }
    auto issue_w1 = [&](int c, int ii) {
      int k = c % 7 + rot;
      k = k >= 7 ? k - 7 : k;
      glds(rs_win, L + RING + (c % 6) * 16384 + w * 2048 + ii * 1024, w_voff[ii], k * 64 * 3584 + ((c < 7 ? 896 : 0) + 128 * tn) * 2);
    };
    auto issue_w = [&](int c) { issue_w1(c, 0); issue_w1(c, 1); };
    issue_w(0); issue_w(1); issue_w(2); issue_w(3);
    v4u raw[4][4];
    int a_voff = (lane >> 2) * 4096 + (lane & 3) * 16;
    auto issue_raw = [&](auto I) { raw[I.v >> 2][I.v & 3] = ldg<(I.v & 3) * 64>(rs_wsp, a_voff, (tm * 64 + 16 * (I.v >> 2)) * 4096 + w * 512); };
    const int d_voff0 = (lane >> 3) * 4096 + (((lane & 7) ^ (lane >> 4)) << 4), d_voff1 = d_voff0 ^ 64;
    auto issue_a = [&](LAS char* dst, int i, int byte) { glds(rs_wsp, dst, (i & 1) ? d_voff1 : d_voff0, (tm * 64 + 8 * i) * 4096 + w * 512 + byte); };
    v4u wo_cp;
    const int wo_ir = tm * 28 + (w < 7 ? w : 6) * 4 + ((lane >> 3) & 3);
    int wo_src, touch_off;
    {
      const int pos = wo_ir >> 4, late = pos >= 28 ? 1 : 0, e = pos - 28 * late;
      const int f = ((e >> 2) * 8 + late * 4 + (e & 3)) * 16 + (wo_ir & 15);
      const int ch = (lane & 7) ^ (((wo_ir >> 1) & 1) << 2);
      wo_src = f * 896 + (ch >> 2) * 448 + (ch & 3) * 16;
      const int l1 = (tn * 8 + w) * 37 + (lane < 37 ? lane : 36);
      touch_off = (l1 < 2048 ? l1 : 2047) * 128;
    }
    const int frow = 32 * mw + r;
    int xa[4], wa[2][2], xw[7];
#pragma unroll
    for (int k = 0; k < 4; ++k) xa[k] = X_OFF + frow * 128 + (((2 * k + h) ^ ((frow >> 1) & 7)) << 4);
#pragma unroll
    for (int t = 0; t < 2; ++t) {
      wa[t][0] = RING + (8 * h + 4 * t + tq) * 256 + (((4 * nw + 2 * tblk + (tp >> 1)) ^ ((tq << 2) | ((2 * h + t) & 3))) << 4) + 8 * (tp & 1);
      wa[t][1] = wa[t][0] + 49152;
    }
#pragma unroll
    for (int i = 0; i < 7; ++i) {
      const int pos = i - rot < 0 ? i - rot + 7 : i - rot;
      xw[i] = X_OFF + xrow * 128 + (((lane & 7) ^ ((xrow >> 1) & 7)) << 4) + pos * 8192;
    }
    asm volatile("" : "+v"(xa[0]), "+v"(xa[1]), "+v"(xa[2]), "+v"(xa[3]), "+v"(wa[0][0]), "+v"(wa[0][1]), "+v"(wa[1][0]), "+v"(wa[1][1]),
                      "+v"(wo_src), "+v"(touch_off), "+v"(a_voff));
    asm volatile("" : "+v"(xw[0]), "+v"(xw[1]), "+v"(xw[2]), "+v"(xw[3]), "+v"(xw[4]), "+v"(xw[5]), "+v"(xw[6]));
    const unsigned tag = load_tag(ws);
    {
      float s = 0.f, ss = 0.f;
      sfor<0, 7>([&](auto I) {
        constexpr int i = I.v;
        wait_vm<14 - i>();
        pin(xr[i]);
#pragma unroll
        for (int j = 0; j < 4; ++j) { s = dot2(xr[i][j], ONES, s); ss = dot2(xr[i][j], xr[i][j], ss); }
      });
      const f32x2 xst = ln_consts(sum8(s), sum8(ss), 1.0f / 448.0f);
      const float nm = -xst[0];
#pragma unroll
      for (int i = 0; i < 7; ++i) {
        *(LAS v4u*)(L + xw[i]) = add8(xr[i], nm);
        if (i >= 3) issue_w1(4 + ((i - 3) >> 1), (i - 3) & 1);
      }
      if ((lane & 7) == 0) *(LAS float*)(L + XST + xrow * 4) = xst[1];
      if (tid < 4) *(LAS unsigned*)(L + CNT + 4 * tid) = 0u;
    }
    wo_cp = ldg<0>(rs_wout, wo_src, tn * 64);
    touch(rs_wsp, L + JUNK, touch_off, tm * 64 * 4096);
    f32x16 acc;
    f32x2 z2[8];
    v2u sg[7];
    v4u xf[2][4], wf[2][4];
    auto read_frag = [&](int c, int k) {
      xf[c & 1][k] = *(LAS v4u*)(L + xa[k] + (c % 7) * 8192);
      wf[c & 1][k] = cat(tr_read(L + wa[0][(c % 6) / 3] + (c % 3) * 16384 + k * 4096), tr_read(L + wa[1][(c % 6) / 3] + (c % 3) * 16384 + k * 4096));
    };
    wait_vm<10 + P1_PRE>();
    wg_barrier();
#pragma unroll
    for (int k = 0; k < 4; ++k) read_frag(0, k);
    const f32x2 xri = splat(*(LAS float*)(L + XST + frow * 4));
    sfor<0, 14>([&](auto C) {
      constexpr int c = C.v;
      wait_vm<p1_wait(c)>();
      wg_barrier();
      if constexpr (c == IT_FLAG) {
        if (tid == 0) __hip_atomic_store(wsw + (WS_F1 / 4) + id * 32, tag, RLX_AGENT);
      }
      if constexpr (c == 7) {
        if (w == 7) {
          f32x2 s = *(LAS f32x2*)(L + STP + lane * 8);
#pragma unroll
          for (int q = 1; q < 4; ++q) s += *(LAS f32x2*)(L + STP + q * 512 + lane * 8);
          v2u g0 = {as_u(s[0]), tag}, g1 = {as_u(s[1]), tag};
          st64_sc1(g0, rs_ws, lane * 8, WS_ST + id * 1024);
          st64_sc1(g1, rs_ws, lane * 8 + 512, WS_ST + id * 1024);
        }
      }
      sfor<0, 4>([&](auto K) {
        constexpr int k = K.v;
        SCHED_FENCE();
        acc = mfma_bf(wf[c & 1][k], xf[c & 1][k], (c % 7 == 0 && k == 0) ? zero16() : acc);
        SCHED_FENCE();
        if constexpr (c < 13) read_frag(c + 1, k);
        if constexpr (k == 0 || k == 2) {
          if constexpr (c <= 7) issue_w1(c + 6, k >> 1);
          if constexpr (c >= 8 && c <= 11) issue_a(L + A3_OFF + (c - 8) * 16384 + w * 2048 + (k >> 1) * 1024, 2 * (c - 8) + (k >> 1), 384);
        }
        if constexpr (c != 6 && k == 3) sfor<0, p1_raw(c)>([&](auto I) { issue_raw(IC<raw_base(c) + I.v>{}); });
      });
      asm volatile("" : "+v"(acc));
      SCHED_FENCE();
      if constexpr (c == IT_SG) {
#pragma unroll
        for (int q = 0; q < 7; ++q) sg[q] = ldg64_sc1(rs_wsd, h * 512 + frow * 8, WS_ST + (tm * 7 + q) * 1024);
      }
      if constexpr (c == 6) {
        f32x2 s = splat(0.f), ss = splat(0.f);
        sfor<0, 8>([&](auto J) {
          z2[J.v] = gelu2(pair<J.v>(acc) * xri);
          s += z2[J.v];
          ss = fma2(z2[J.v], z2[J.v], ss);
          if constexpr (J.v & 1) {
            SCHED_FENCE();
            issue_raw(IC<raw_base(6) + J.v - 1>{});
            issue_raw(IC<raw_base(6) + J.v>{});
            SCHED_FENCE();
          }
        });
        pin(wo_cp);
        st_sc1(wo_cp, rs_ws, wo_ir * 128 + (lane & 7) * 16, WS_WO + tn * 896 * 128);
        f32x2 p = {xhalf_sum(s[0] + s[1]), xhalf_sum(ss[0] + ss[1])};
        if (h == 0) *(LAS f32x2*)(L + STP + nw * 512 + frow * 8) = p;
      }
      if constexpr (c == IT_PUB) {
        wait_vm<2 * (IT_PUB - IT_SG)>();
        float tot = 0.f;
        bool ok = true;
#pragma unroll
        for (int q = 0; q < 7; ++q) { pin(sg[q]); ok &= sg[q][1] == tag; tot += as_f(sg[q][0]); }
        for (unsigned s = 0; !__all(ok) && s < kSpinCap; ++s) {
          ok = true;
          tot = 0.f;
#pragma unroll
          for (int q = 0; q < 7; ++q) {
            const v2u g = ld64_sc1(rs_ws, h * 512 + frow * 8, WS_ST + (tm * 7 + q) * 1024);
            ok &= g[1] == tag;
            tot += as_f(g[0]);
          }
        }
        auto t = __builtin_amdgcn_permlane32_swap(as_u(tot), as_u(tot), false, false);
        const f32x2 st = ln_consts(as_f(t[0]), as_f(t[1]), 1.0f / 896.0f);
        const f32x2 ri = splat(st[1]), mr = splat(-st[0] * st[1]);
        LAS char* const T = L + X_OFF + w * 2048;
#pragma unroll
        for (int g = 0; g < 4; ++g) {
          v2u o = {pk_bf16(fma2(z2[2 * g], ri, mr)), pk_bf16(fma2(z2[2 * g + 1], ri, mr))};
          *(LAS v2u*)(T + r * 64 + ((g ^ ((r >> 2) & 3)) << 4) + 8 * h) = o;
        }
        v4u zo[2];
#pragma unroll
        for (int u = 0; u < 2; ++u) {
          const int row = 8 * h + tq;
          const v2u lo = tr_read(T + u * 1024 + row * 64 + (((2 * tblk + (tp >> 1)) ^ ((2 * h) & 3)) << 4) + 8 * (tp & 1));
          const v2u hi = tr_read(T + u * 1024 + (row + 4) * 64 + (((2 * tblk + (tp >> 1)) ^ ((2 * h + 1) & 3)) << 4) + 8 * (tp & 1));
          zo[u] = cat(lo, hi);
        }
#pragma unroll
        for (int u = 0; u < 2; ++u) st_sc1(zo[u], rs_ws, lane * 16 + u * 1024, WS_Z2N + tn * ZT + nw * ZQ + (tm >> 2) * ZW + (4 * (tm & 3) + 2 * mw) * 1024);
      }
    });
    LAS char* const T = L + Z1_OFF + w * 2048;
    v4u wsp[2][12];
    auto convert = [&](int tb) {
      const int cw = (lane >> 2) * 64 + (((lane & 3) ^ ((lane >> 4) & 3)) << 4);
#pragma unroll
      for (int kp = 0; kp < 4; ++kp) {
        pin(raw[2 * tb][kp]); pin(raw[2 * tb + 1][kp]);
        *(LAS v4u*)(T + cw) = raw[2 * tb][kp];
        *(LAS v4u*)(T + cw + 1024) = raw[2 * tb + 1][kp];
        wsp[tb][2 * kp] = *(LAS v4u*)(T + r * 64 + ((h ^ ((r >> 2) & 3)) << 4));
        wsp[tb][2 * kp + 1] = *(LAS v4u*)(T + r * 64 + (((2 + h) ^ ((r >> 2) & 3)) << 4));
      }
    };

    LAS char* const PW = L + (w < 7 ? w * 8192 : PART7);
#pragma unroll
    for (int i = 0; i < 8; ++i) issue_a(PW + i * 1024, i, 256);
    convert(0);
    const int e_tb = w >> 2, e_u = (w >> 1) & 1, e_slot = 32 * (w & 1) + (lane >> 1), e_a = lane & 1;
    const float bias = bf_lo(b_sp[tm * 64 + 32 * e_tb + (e_slot & 31)]);
    poll_flags(wsw + (WS_F1 / 4) + ((4 * w + (lane & 3)) * 7 + tn) * 32, tag);
    constexpr int WIN = 10;
    auto b_load = [&](int s) { return ld_frag(rs_ws, lane * 16, s & 15, WS_Z2N + tn * ZT + (s >> 4) * ZQ + w * ZW); };
    v4u bw[WIN];
#pragma unroll
    for (int s = 0; s < WIN; ++s) bw[s] = b_load(s);
    SCHED_FENCE();
    convert(1);
    int aa[4];
#pragma unroll
    for (int k = 0; k < 4; ++k) aa[k] = (r & 15) * 128 + (((2 * k + h) ^ ((r >> 1) & 7)) << 4);
#pragma unroll
    for (int k = 0; k < 4; ++k)
#pragma unroll
      for (int tb = 0; tb < 2; ++tb) wsp[tb][8 + k] = *(LAS v4u*)(PW + tb * 4096 + (r >> 4) * 2048 + aa[k]);
#pragma unroll
    for (int k = 0; k < 4; ++k) aa[k] += A3_OFF + (r >> 4) * 16384 + w * 2048;
    SCHED_FENCE();
    f32x16 pa[2][2];
    f32x4 rsum, ra, rb;
    v4u a3[2][2];
    const int red_w = lane << 4, red_w1 = (lane ^ 8) << 4;
    const int red_r = (e_tb * 4 + 2 * e_u + e_a) * 1024 + ((e_slot ^ (8 * e_a)) << 4);
    auto red_write = [&](int p, int i) {
      const f32x16& a = pa[p & 1][i >> 2];
      const int g = i & 3;
      f32x4 v = {a[4 * g], a[4 * g + 1], a[4 * g + 2], a[4 * g + 3]};
      *(LAS f32x4*)(PW + i * 1024 + ((g & 1) ? red_w1 : red_w)) = v;
    };
    auto red_read = [&](int i) { return *(LAS f32x4*)(L + (i < 7 ? i * 8192 : PART7) + red_r); };
    auto red_finish = [&](int q) {
      const v2u z1 = *(LAS v2u*)(L + Z1_OFF + (q * 2 + e_tb) * 2048 + e_u * 1024 + e_slot * 16 + e_a * 8);
      v2u o;
#pragma unroll
      for (int j = 0; j < 2; ++j) o[j] = pk_bf16(bf_lo(z1[j]) * (rsum[2 * j] + bias), bf_hi(z1[j]) * (rsum[2 * j + 1] + bias));
      st64_sc1(o, rs_ws, e_slot * 16 + e_a * 8, WS_H + ((tm * 2 + e_tb) * 56 + (q >> 1) * 28 + tn * 4 + (q & 1) * 2 + e_u) * 1024);
    };
    v4u wo[14];
    v4u z1v;
    sfor<0, 64>([&](auto S) {
      constexpr int s = S.v, q = s >> 4, ks = s & 15;
      if constexpr (ks < 12) {
        pa[q & 1][0] = mfma_bf(bw[s % WIN], wsp[0][ks], ks == 0 ? zero16() : pa[q & 1][0]);
        pa[q & 1][1] = mfma_bf(bw[s % WIN], wsp[1][ks], ks == 0 ? zero16() : pa[q & 1][1]);
      } else {
        pa[q & 1][0] = mfma_bf(bw[s % WIN], a3[ks & 1][0], pa[q & 1][0]);
        pa[q & 1][1] = mfma_bf(bw[s % WIN], a3[ks & 1][1], pa[q & 1][1]);
      }
      SCHED_FENCE();
      if constexpr (s + WIN < 64) bw[s % WIN] = b_load(s + WIN);
      if constexpr (s >= WO0) wo[s - WO0] = ld_frag(rs_ws, lane * 16, s - WO0, WS_WO + tn * 896 * 128 + w * 14336);
      if constexpr (q == 0) {
        float t = acc[ks] * xri[0];
        float u = t * t;
        asm volatile("" : "+v"(u));
        u = t * __builtin_fmaf(u, -2.302208198f * 0.044715f, -2.302208198f);
        t *= __builtin_amdgcn_rcpf(1.0f + __builtin_amdgcn_exp2f(u));
        asm volatile("" : "+v"(t));
        acc[ks] = t;
        if constexpr (ks & 1) z1v[(ks >> 1) & 3] = pk_bf16(acc[ks - 1], acc[ks]);
        if constexpr ((ks & 7) == 7) *(LAS v4u*)(T + (ks >> 3) * 1024 + lane * 16) = z1v;
      }
      if constexpr (ks >= 10 && ks < 14) {
        a3[ks & 1][0] = *(LAS v4u*)(L + aa[ks - 10]);
        a3[ks & 1][1] = *(LAS v4u*)(L + aa[ks - 10] + 32768);
      }
      if constexpr (q >= 1) {
        if constexpr (ks < 8) red_write(q - 1, ks);
        if constexpr (ks == 8) {
          if constexpr (q == 3) wait_vm<p2_early_wait(WIN)>();
          wg_barrier();
          if constexpr (q == 3) { if (tid == 0) __hip_atomic_store(wsw + (WS_F3 / 4) + id * 32, tag, RLX_AGENT); }
        }
        if constexpr (ks >= 10 && ks < 14) {
#pragma unroll
          for (int j = 0; j < 4; ++j) {
            float t = ra[j] + rb[j];
            if constexpr (ks > 10) t += rsum[j];
            asm volatile("" : "+v"(t));
            rsum[j] = t;
          }
        }
        if constexpr (ks >= 9 && ks < 13) { ra = red_read(2 * (ks - 9)); rb = red_read(2 * (ks - 9) + 1); }
        if constexpr (ks == 14) red_finish(q - 1);
        if constexpr (ks == 15) wg_barrier();
      }
      SCHED_FENCE();
    });
    const int p_tb = w & 1, p_g = w >> 1, c0 = 15 * p_tb;
    gu32* early_flag;
    {
      const int c = c0 + (lane % 15);
      early_flag = w < 6 ? wsw + (WS_F3 / 4) + (tm * 7 + (lane % 7)) * 32 : wsw + (WS_F2 / 4) + (224 + (c > 28 ? 28 : c)) * 32;
    }
    const unsigned ef = __hip_atomic_load(early_flag, RLX_AGENT);
#pragma unroll
    for (int i = 0; i < 8; ++i) red_write(3, i);
#pragma unroll
    for (int i = 64 - WO0; i < 14; ++i) wo[i] = ld_frag(rs_ws, lane * 16, i, WS_WO + tn * 896 * 128 + w * 14336);
    wg_barrier();
    rsum = red_read(0);
#pragma unroll
    for (int i = 1; i < 8; ++i) rsum += red_read(i);
    const bool early = __all(ef == tag);
    red_finish(3);
    wait_vm<0>();
    wg_barrier();
    if (tid == 0) __hip_atomic_store(wsw + (WS_F2 / 4) + id * 32, tag, RLX_AGENT);

    const int e_t = tid >> 3, e_q = tid & 7;
    const size_t e_off = (size_t)(tm * 64 + e_t) * 896 + (e_q >> 2) * 448 + tn * 64 + (e_q & 3) * 16;
    constexpr int YP_OFF = 114688;
    const int k0 = p_g == 0 ? 0 : 1 + 9 * p_g, k1 = 28 + 9 * p_g;
    const int e_so = w < 6 ? WS_H + ((tm * 2 + p_tb) * 56 + k0) * 1024 : WS_Y + (tm * 29 + c0) * 2048;
    auto image_write = [&]() {
#pragma unroll
      for (int i = 0; i < 14; ++i) *(LAS v4u*)(L + (w * 14 + i) * 1024 + lane * 16) = wo[i];
      lds_arrive(L + CNT, lane);
    };
    f32x16 c2[2];
    v4u xres;
    if (w < 6) {
      if (!early) poll_flags(early_flag, tag);
      __builtin_amdgcn_fence(__ATOMIC_ACQUIRE, "wavefront");
      v4u ea_[10];
#pragma unroll
      for (int i = 0; i < 10; ++i)
        if (i < 9 || p_g == 0) ea_[i] = ld_frag(rs_ws, lane * 16, i, e_so);
      xres = *(const GAS v4u*)((const GAS char*)x + e_off);
      SCHED_FENCE();
      image_write();
      lds_wait(L + CNT, 8u);
      gu32* const late_flag = wsw + (WS_F2 / 4) + (tm * 7 + (lane % 7)) * 32;
      unsigned lf = 0;
      int p[2];
#pragma unroll
      for (int db = 0; db < 2; ++db)
        p[db] = (4 * h + tq) * 128 + (((4 * db + 2 * tblk + (tp >> 1)) ^ (((tq >> 1) & 1) << 2)) << 4) + 8 * (tp & 1);
#pragma unroll
      for (int i = 0; i < 10; ++i) {
        if (i == 5) lf = __hip_atomic_load(late_flag, RLX_AGENT);
        if (i < 9 || p_g == 0) {
#pragma unroll
          for (int db = 0; db < 2; ++db) {
            LAS char* a = L + k0 * 2048 + p[db] + i * 2048;
            c2[db] = mfma_bf(cat(tr_read(a), tr_read(a + 1024)), ea_[i], i == 0 ? zero16() : c2[db]);
          }
        }
      }
      asm volatile("" : "+v"(c2[0]), "+v"(c2[1]));
      lds_arrive(L + CNT + 4 + 4 * p_g, lane);
      if (!__all(lf == tag)) poll_flags(late_flag, tag);
      __builtin_amdgcn_fence(__ATOMIC_ACQUIRE, "wavefront");
      v4u hl[10], wl[10][2];
#pragma unroll
      for (int i = 0; i < 10; ++i)
        if (i < 9 || p_g == 2) hl[i] = ld_frag(rs_ws, lane * 16, i, WS_H + ((tm * 2 + p_tb) * 56 + k1) * 1024);
      SCHED_FENCE();
#pragma unroll
      for (int i = 0; i < 10; ++i)
#pragma unroll
        for (int db = 0; db < 2; ++db) {
          LAS char* a = L + k1 * 2048 + p[db] + i * 2048;
          wl[i][db] = cat(tr_read(a), tr_read(a + 1024));
        }
      SCHED_FENCE();
#pragma unroll
      for (int i = 0; i < 10; ++i) {
        if (i < 9 || p_g == 2) {
          c2[0] = mfma_bf(wl[i][0], hl[i], c2[0]);
          c2[1] = mfma_bf(wl[i][1], hl[i], c2[1]);
        }
      }
      lds_wait(L + CNT + 4 + 4 * p_g, 2u);
    } else {
      image_write();
      if (!early) poll_flags(early_flag, tag);
      __builtin_amdgcn_fence(__ATOMIC_ACQUIRE, "wavefront");
      v4u ea_[30];
#pragma unroll
      for (int i = 0; i < 30; ++i)
        if (i < 28 || w == 6) ea_[i] = ld_frag(rs_ws, lane * 16, i, e_so);
      xres = *(const GAS v4u*)((const GAS char*)x + e_off);
      SCHED_FENCE();
      const float th = (float)(tn * 32 + r) * (1.0f / 448.0f);
      const float c16 = 2.0f * cosr(16.0f * th);
      float ea[8], eb[8];
#pragma unroll
      for (int q = 0; q < 4; ++q) {
        const float k = (float)(8 * h + 2 * q + p_tb);
        ea[2 * q] = cosr(k * th); ea[2 * q + 1] = -sinr(k * th);
        eb[2 * q] = cosr((k + 16.0f) * th); eb[2 * q + 1] = -sinr((k + 16.0f) * th);
      }
#pragma unroll
      for (int i = 0; i < 15; ++i) {
        float(&e)[8] = (i & 1) ? eb : ea;
        float(&o)[8] = (i & 1) ? ea : eb;
        const v4u gf = {pk_bf16(e[0], e[1]), pk_bf16(e[2], e[3]), pk_bf16(e[4], e[5]), pk_bf16(e[6], e[7])};
        if (i < 14 || w == 6) {
          c2[0] = mfma_bf(gf, ea_[2 * i], i == 0 ? zero16() : c2[0]);
          c2[1] = mfma_bf(gf, ea_[2 * i + 1], i == 0 ? zero16() : c2[1]);
        }
        if (i < 13) {
#pragma unroll
          for (int q = 0; q < 8; ++q) e[q] = opq(__builtin_fmaf(c16, o[q], -e[q]));
        }
      }
    }
    {
      LAS char* const P = L + (w < 6 ? k0 * 2048 : YP_OFF) + p_tb * 8192;
#pragma unroll
      for (int b = 0; b < 2; ++b)
#pragma unroll
        for (int g = 0; g < 4; ++g) {
          f32x4 v = {c2[b][4 * g], c2[b][4 * g + 1], c2[b][4 * g + 2], c2[b][4 * g + 3]};
          *(LAS f32x4*)(P + (b * 4 + g) * 1024 + ((lane ^ ((b * 4 + g) << 1)) << 4)) = v;
        }
    }
    wg_barrier();
    {
      const int tb = e_t >> 5, db = e_q >> 2, g = e_q & 3;
      const float sgn = db ? -1.0f : 1.0f;
      v4u o;
#pragma unroll
      for (int hh = 0; hh < 2; ++hh) {
        const int sl = (e_t & 31) + 32 * hh;
        const int oh = tb * 8192 + e_q * 1024 + ((sl ^ (e_q << 1)) << 4);
        const int oy = YP_OFF + (tb * 4 + g) * 1024 + ((sl ^ ((tb * 4 + g) << 1)) << 4);
        const f32x4 so = *(LAS f32x4*)(L + oh) + *(LAS f32x4*)(L + oh + 20480) + *(LAS f32x4*)(L + oh + 38912);
        const f32x4 ye = *(LAS f32x4*)(L + oy), yo = *(LAS f32x4*)(L + oy + 8192);
#pragma unroll
        for (int j = 0; j < 2; ++j) {
          const f32x2 ov = {so[2 * j], so[2 * j + 1]};
          const f32x2 e2 = {ye[2 * j], ye[2 * j + 1]}, o2 = {yo[2 * j], yo[2 * j + 1]};
          const f32x2 sv = fma2(o2, splat(sgn), e2);
          const f32x2 gv = sigm_mul(ov, ov * splat(-1.44269504f));
          o[2 * hh + j] = pk_bf16(fma2(sv, gv, unpk(xres[2 * hh + j])));
        }
      }
      *(GAS v4u*)((GAS char*)out + e_off) = o;
    }
  } else {
    const int j = xcd * 4 + (rank - 28);
    constexpr int TW = 0, TAB = 16384, X_OFF = 32768, WRAW = 98304, WSZ = 29696;
    const v4u rs_wr = mk_desc(w_r, 2048 * 225 * 2);
    const v4u rs_wi = mk_desc(w_i, 2048 * 225 * 2);
    const int lr = 8 * w + (lane >> 3), slot = lane & 7;
    v4u xa_[4], xb_[4];
    {
      const int rho = lr & 31;
      const int m = (rho & 19) | ((rho & 4) << 1) | ((rho & 8) >> 1);
      const int voff = (2 * m + (lr >> 5)) * 896 + slot * 16, voff3 = (2 * m + (lr >> 5)) * 896 + (slot & 3) * 16;
      sfor<0, 4>([&](auto I) {
        xa_[I.v] = ldg<I.v * 128>(rs_x, I.v == 3 ? voff3 : voff, j * 64 * 896);
        xb_[I.v] = ldg<I.v * 128 + 448>(rs_x, I.v == 3 ? voff3 : voff, j * 64 * 896);
      });
    }
#pragma unroll
    for (int i = 0; i < 4; ++i) {
      const int p = w * 4 + i > 28 ? 28 : w * 4 + i;
      const int vo = (p == 28 ? (lane & 7) : lane) * 16;
      glds(rs_wr, L + WRAW + p * 1024, vo, j * 28800 + p * 1024);
      glds(rs_wi, L + WRAW + WSZ + p * 1024, vo, j * 28800 + p * 1024);
    }
    const int par = w >> 2, cb0 = 2 * (w & 3);
    float ea[2][8], eb[2][8], c16[2];
#pragma unroll
    for (int cb = 0; cb < 2; ++cb) {
      const int col = 32 * (cb0 + cb) + r;
      const bool nyq = par == 0 && col == 1;
      const float th = (float)(nyq ? 224 : (col & ~1) + par) * (1.0f / 448.0f);
      const float ph = (!nyq && (col & 1)) ? 0.25f : 0.0f;
      c16[cb] = 2.0f * cosr(16.0f * th);
#pragma unroll
      for (int q = 0; q < 8; ++q) {
        ea[cb][q] = cosr(__builtin_fmaf((float)(8 * h + q), th, ph));
        eb[cb][q] = cosr(__builtin_fmaf((float)(16 + 8 * h + q), th, ph));
        asm volatile("" : "+v"(ea[cb][q]), "+v"(eb[cb][q]));
      }
    }
    const unsigned tag = load_tag(ws);
    {
      f32x2 sv[4][4], tv[4][4];
      f32x2 s = splat(0.f), ss = splat(0.f);
      sfor<0, 4>([&](auto I) {
        constexpr int i = I.v;
        wait_vm<8 + 6 - 2 * i>();
        pin(xa_[i]); pin(xb_[i]);
        if constexpr (i == 3) { s += s; ss += ss; }
#pragma unroll
        for (int q = 0; q < 4; ++q) {
          const f32x2 a = unpk(xa_[i][q]), b = unpk(xb_[i][q]);
          sv[i][q] = a + b;
          tv[i][q] = a - b;
          s += sv[i][q];
          ss = fma2(sv[i][q], sv[i][q], ss);
          ss = fma2(tv[i][q], tv[i][q], ss);
        }
      });
      const f32x2 st = ln_consts(sum8(s[0] + s[1]) * 0.5f, sum8(ss[0] + ss[1]) * 0.25f, 1.0f / 448.0f);
      const float rr = st[1] * 0.0625f;
      const f32x2 ri = splat(rr), mr = splat(-2.0f * st[0] * rr);
      const int xw = X_OFF + lr * 128 + ((slot ^ ((lr >> 1) & 7)) << 4);
#pragma unroll
      for (int i = 0; i < 4; ++i) {
        v4u os, ot;
#pragma unroll
        for (int q = 0; q < 4; ++q) {
          os[q] = pk_f16(fma2(sv[i][q], ri, mr));
          ot[q] = pk_f16(tv[i][q] * ri);
        }
        *(LAS v4u*)(L + xw + i * 8192) = os;
        *(LAS v4u*)(L + xw + i * 8192 + 32768) = ot;
      }
    }
    wait_vm<0>();
    wg_barrier();
    f32x16 xd[2][2];
    {
      int xa[4];
#pragma unroll
      for (int k = 0; k < 4; ++k) xa[k] = X_OFF + par * 32768 + r * 128 + (((2 * k + h) ^ ((r >> 1) & 7)) << 4);
      v4u xq[2][2];
      xq[0][0] = *(LAS v4u*)(L + xa[0]);
      xq[0][1] = *(LAS v4u*)(L + xa[0] + 4096);
      auto step = [&](auto KS, auto NCB) {
        constexpr int ks = KS.v;
        if constexpr (ks < 13) {
          xq[(ks + 1) & 1][0] = *(LAS v4u*)(L + xa[(ks + 1) & 3] + ((ks + 1) >> 2) * 8192);
          xq[(ks + 1) & 1][1] = *(LAS v4u*)(L + xa[(ks + 1) & 3] + ((ks + 1) >> 2) * 8192 + 4096);
        }
        SCHED_FENCE();
#pragma unroll
        for (int cb = 0; cb < NCB.v; ++cb) {
          float(&e)[8] = (ks & 1) ? eb[cb] : ea[cb];
          float(&o)[8] = (ks & 1) ? ea[cb] : eb[cb];
          v4u f = {pk_f16(e[0], e[1]), pk_f16(e[2], e[3]), pk_f16(e[4], e[5]), pk_f16(e[6], e[7])};
          xd[cb][0] = mfma_h(xq[ks & 1][0], f, ks == 0 ? zero16() : xd[cb][0]);
          xd[cb][1] = mfma_h(xq[ks & 1][1], f, ks == 0 ? zero16() : xd[cb][1]);
          if constexpr (ks < 12) {
#pragma unroll
            for (int q = 0; q < 8; ++q) e[q] = opq(__builtin_fmaf(c16[cb], o[q], -e[q]));
          }
        }
        SCHED_FENCE();
      };
      auto w_groups = [&](int g0, auto N) {
        const int ksub = lane >> 4, kp = lane & 15;
        LAS char* src = L + WRAW + kp * 900 + (4 * g0 + ksub) * 2;
        v4u o[N.v];
#pragma unroll
        for (int g = 0; g < N.v; ++g)
#pragma unroll
          for (int q = 0; q < 4; ++q) {
            const unsigned lo = *(LAS unsigned short*)(src + g * 8 + (q >> 1) * 450 + (q & 1) * WSZ);
            const unsigned hi = *(LAS unsigned short*)(src + g * 8 + (q >> 1) * 450 + (q & 1) * WSZ + 14400);
            o[g][q] = lo | (hi << 16);
          }
#pragma unroll
        for (int g = 0; g < N.v; ++g) st_sc1(o[g], rs_ws, ksub * WTS + kp * 16, WS_WT + (g0 + g) * 4 * WTS + j * 256);
      };
      if ((w & 3) != 3) {
        w_groups((w - par) * 3, IC<3>{});
        sfor<0, 14>([&](auto KS) { step(KS, IC<2>{}); });
      } else {
#pragma unroll 1
        for (int bt = 0; bt < 3; ++bt) w_groups(18 + 21 * par + 7 * bt, IC<7>{});
        sfor<0, 14>([&](auto KS) { step(KS, IC<1>{}); });
      }
    }
    wait_vm<0>();
    wg_barrier();
    if (tid == 0) __hip_atomic_store(wsw + (WS_F3 / 4) + (224 + j) * 32, tag, RLX_AGENT);
#pragma unroll
    for (int cb = 0; cb < 2; ++cb) {
      if (cb == 0 || (w & 3) != 3) {
        LAS char* T = L + X_OFF + (2 * w + cb) * 4096;
#pragma unroll
        for (int rb = 0; rb < 2; ++rb)
#pragma unroll
          for (int u = 0; u < 2; ++u) {
            v4u o;
#pragma unroll
            for (int q = 0; q < 4; ++q) o[q] = pk_f16(xd[cb][rb][8 * u + 2 * q], xd[cb][rb][8 * u + 2 * q + 1]);
            *(LAS v4u*)(T + r * 128 + (((4 * rb + 2 * u + h) ^ (r & 7)) << 4)) = o;
          }
        v4u v[4];
#pragma unroll
        for (int i = 0; i < 4; ++i) v[i] = *(LAS v4u*)(T + i * 1024 + (lane >> 3) * 128 + (((lane & 7) ^ (lane >> 3)) << 4));
#pragma unroll
        for (int i = 0; i < 4; ++i)
          st_sc1(v[i], rs_ws, (lane >> 3) * XDS + (lane & 7) * 16, WS_XD + (par * 224 + 32 * (cb0 + cb) + 8 * i) * XDS + j * 128);
      }
    }
    const unsigned wflag = __hip_atomic_load(wsw + (WS_F3 / 4) + (224 + r) * 32, RLX_AGENT);
#pragma unroll
    for (int i = 0; i < 2; ++i) {
      const int e = w * 2 + i, which = e >> 3, blk = (e >> 2) & 1, rq = e & 3;
      const int cr = 8 * rq + 4 * h;
      const float a0 = (float)(which ? (2 * r + blk) * cr : (2 * cr + blk) * r) * (1.0f / 2048.0f);
      const float da = (float)(which ? 2 * r + blk : 2 * r) * (1.0f / 2048.0f);
      float c[4], sn[4];
#pragma unroll
      for (int q = 0; q < 4; ++q) { const float a = __builtin_fmaf((float)q, da, a0); c[q] = 0.125f * cosr(a); sn[q] = 0.125f * sinr(a); }
      v4u v = {pk_f16(c[0], c[1]), pk_f16(sn[0], sn[1]), pk_f16(c[2], c[3]), pk_f16(sn[2], sn[3])};
      *(LAS v4u*)(L + TW + e * 1024 + lane * 16) = v;
    }
    if (w < 6) {
      const int t = w >> 1, sx = w & 1;
      v4u c, sn;
#pragma unroll
      for (int q = 0; q < 4; ++q) {
        const int k0 = 16 * sx + 8 * (q >> 1) + 4 * h + 2 * (q & 1);
        const int m0 = t == 0 ? 2 * r * k0 : t == 1 ? r * (2 * k0 + 1) : k0 * (2 * r + 1);
        const int dm = t == 2 ? 2 * r + 1 : 2 * r;
        const float a0 = (float)m0 * (1.0f / 64.0f), a1 = (float)(m0 + dm) * (1.0f / 64.0f);
        c[q] = pk_f16(cosr(a0), cosr(a1));
        sn[q] = pk_f16(sinr(a0), sinr(a1));
      }
      *(LAS v4u*)(L + TAB + w * 2048 + lane * 16) = c;
      *(LAS v4u*)(L + TAB + w * 2048 + 1024 + lane * 16) = sn;
    }
    wait_vm<0>();
    const bool wready = __all(wflag == tag);
    wg_barrier();
    if (tid == 0) __hip_atomic_store(wsw + (WS_F1 / 4) + (224 + j) * 32, tag, RLX_AGENT);

    if (j < 29) {
      const int kpar = j >= 15 ? 1 : 0, ku = 8 * (j - 15 * kpar) + w, k = 2 * ku + kpar;
      if (!wready) poll_flags(wsw + (WS_F3 / 4) + (224 + r) * 32, tag);
      __builtin_amdgcn_fence(__ATOMIC_ACQUIRE, "wavefront");
      v2u wv[2][8];
#pragma unroll
      for (int ab = 0; ab < 2; ++ab)
#pragma unroll
        for (int rp = 0; rp < 8; ++rp)
          wv[ab][rp] = ld64_sc1(rs_ws, r * 8 + h * 512 + ((rp & 1) + 4 * (rp >> 1)) * 256, WS_WT + k * WTS + ab * 4096);
      SCHED_FENCE();
      LAS char* const IO = L + X_OFF + w * 8192;
      unsigned yo[2][16];
      if (k <= 224) {
        poll_flags(wsw + (WS_F1 / 4) + (224 + r) * 32, tag);
        const bool realcol = (k == 0 || k == 224);
        const int pre = k == 224 ? 1 : kpar * 224 + 2 * ku;
        v4u in[8];
#pragma unroll
        for (int i = 0; i < 8; ++i) in[i] = ld_sc1(rs_ws, lane * 16 + (i & 3) * 1024, WS_XD + (pre + (i >> 2)) * XDS);
        SCHED_FENCE();
        float mx = 0.f;
#pragma unroll
        for (int ab = 0; ab < 2; ++ab)
#pragma unroll
          for (int rp = 0; rp < 8; ++rp)
#pragma unroll
            for (int q = 0; q < 2; ++q)
              mx = max3_abs(mx, as_f(wv[ab][rp][q]), bf_lo(wv[ab][rp][q]));
        int ex = (int)(wave_max(as_u(mx)) >> 23);
        ex = ex < 16 ? 16 : (ex > 240 ? 240 : ex);
        const f32x2 wsc = splat(as_f((unsigned)(127 + 126 - 3 - ex) << 23));
#pragma unroll
        for (int ab = 0; ab < 2; ++ab)
#pragma unroll
          for (int rp = 0; rp < 8; ++rp) {
            wv[ab][rp][0] = pk_f16(unpk(wv[ab][rp][0]) * wsc);
            wv[ab][rp][1] = pk_f16(unpk(wv[ab][rp][1]) * wsc);
            pin(wv[ab][rp]);
          }
        SCHED_FENCE();
        v4u FC[2], FS[2], FN[2], AC[2], AS_[2], AN[2], BC[2], BS[2], BN[2];
#pragma unroll
        for (int s = 0; s < 2; ++s) {
          LAS char* t = L + TAB + s * 2048 + lane * 16;
          FC[s] = *(LAS v4u*)t; FS[s] = *(LAS v4u*)(t + 1024); FN[s] = FS[s] ^ 0x80008000u;
          AC[s] = *(LAS v4u*)(t + 4096); AS_[s] = *(LAS v4u*)(t + 5120); AN[s] = AS_[s] ^ 0x80008000u;
          BC[s] = *(LAS v4u*)(t + 8192); BS[s] = *(LAS v4u*)(t + 9216); BN[s] = BS[s] ^ 0x80008000u;
        }
#pragma unroll
        for (int s = 0; s < 2; ++s) { pin(FN[s]); pin(AN[s]); pin(BN[s]); }
        {
          const int row = lane >> 3;
          LAS char* dst = IO + row * 128 + (((lane & 7) ^ (((row >> 1) & 1) << 2)) << 4);
#pragma unroll
          for (int i = 0; i < 4; ++i) *(LAS v4u*)(dst + i * 1024) = in[i];
          if (realcol) {
            v4u z = {0u, 0u, 0u, 0u};
#pragma unroll
            for (int i = 4; i < 8; ++i) in[i] = z;
          }
#pragma unroll
          for (int i = 4; i < 8; ++i) *(LAS v4u*)(dst + i * 1024) = in[i];
        }
        f32x16 re[2], im[2];
        v4u pr[2][2], pi[2][2];
        auto twiddle_pack = [&](int toff, int sgn) {
#pragma unroll
          for (int b = 0; b < 2; ++b)
#pragma unroll
            for (int rq = 0; rq < 4; ++rq) {
              const v4u t = *(LAS v4u*)(L + toff + (b * 4 + rq) * 1024 + lane * 16);
#pragma unroll
              for (int q = 0; q < 2; ++q) {
                const int rp = 2 * rq + q;
                const f16x2 a = as_h2(pk_f16(re[b][2 * rp], re[b][2 * rp + 1])), d = as_h2(pk_f16(im[b][2 * rp], im[b][2 * rp + 1]));
                const f16x2 c = as_h2(t[2 * q]), s = as_h2(t[2 * q + 1]);
                pr[b][rp >> 2][rp & 3] = as_u(hfma(a, c, sgn > 0 ? -(d * s) : d * s));
                pi[b][rp >> 2][rp & 3] = as_u(hfma(d, c, sgn > 0 ? a * s : -(a * s)));
              }
            }
        };
#pragma unroll
        for (int b = 0; b < 2; ++b)
#pragma unroll
          for (int s = 0; s < 2; ++s) {
            const int ch = (4 * b + 2 * tblk + (tp >> 1)) ^ (((tq >> 1) & 1) << 2);
            LAS char* p = IO + (16 * s + 4 * h + tq) * 128 + (ch << 4) + 8 * (tp & 1);
            pr[b][s] = cat(tr_read(p), tr_read(p + 1024));
            pi[b][s] = cat(tr_read(p + 4096), tr_read(p + 4096 + 1024));
          }
#pragma unroll
        for (int b = 0; b < 2; ++b)
#pragma unroll
          for (int s = 0; s < 2; ++s) {
            re[b] = mfma_h(pr[b][s], FC[s], s == 0 ? zero16() : re[b]);
            im[b] = mfma_h(pi[b][s], FC[s], s == 0 ? zero16() : im[b]);
            re[b] = mfma_h(pi[b][s], FS[s], re[b]);
            im[b] = mfma_h(pr[b][s], FN[s], im[b]);
          }
        twiddle_pack(TW, -1);
#pragma unroll
        for (int s = 0; s < 2; ++s) {
          re[0] = mfma_h(FC[s], pr[0][s], s == 0 ? zero16() : re[0]);
          im[0] = mfma_h(FC[s], pi[0][s], s == 0 ? zero16() : im[0]);
          re[0] = mfma_h(FS[s], pi[0][s], re[0]);
          im[0] = mfma_h(FN[s], pr[0][s], im[0]);
        }
#pragma unroll
        for (int s = 0; s < 2; ++s) {
          re[1] = mfma_h(AC[s], pr[1][s], s == 0 ? zero16() : re[1]);
          im[1] = mfma_h(AC[s], pi[1][s], s == 0 ? zero16() : im[1]);
          re[1] = mfma_h(AS_[s], pi[1][s], re[1]);
          im[1] = mfma_h(AN[s], pr[1][s], im[1]);
        }
#pragma unroll
        for (int rp = 0; rp < 8; ++rp) {
          const f16x2 er = as_h2(pk_f16(re[0][2 * rp], re[0][2 * rp + 1])), ei = as_h2(pk_f16(im[0][2 * rp], im[0][2 * rp + 1]));
          const f16x2 orr = as_h2(pk_f16(re[1][2 * rp], re[1][2 * rp + 1])), oi = as_h2(pk_f16(im[1][2 * rp], im[1][2 * rp + 1]));
          const f16x2 war = as_h2(wv[0][rp][0]), wai = as_h2(wv[0][rp][1]), wbr = as_h2(wv[1][rp][0]), wbi = as_h2(wv[1][rp][1]);
          const f16x2 ar = er + orr, ai = ei + oi, br = er - orr, bi = ei - oi;
          const f16x2 yar = hfma(ar, war, -(ai * wai)), yai = hfma(ar, wai, ai * war);
          const f16x2 ybr = hfma(br, wbr, -(bi * wbi)), ybi = hfma(br, wbi, bi * wbr);
          pr[0][rp >> 2][rp & 3] = as_u(yar + ybr); pi[0][rp >> 2][rp & 3] = as_u(yai + ybi);
          pr[1][rp >> 2][rp & 3] = as_u(yar - ybr); pi[1][rp >> 2][rp & 3] = as_u(yai - ybi);
        }
#pragma unroll
        for (int s = 0; s < 2; ++s) {
          re[0] = mfma_h(pr[0][s], FC[s], s == 0 ? zero16() : re[0]);
          im[0] = mfma_h(pi[0][s], FC[s], s == 0 ? zero16() : im[0]);
          re[0] = mfma_h(pi[0][s], FN[s], re[0]);
          im[0] = mfma_h(pr[0][s], FS[s], im[0]);
        }
#pragma unroll
        for (int s = 0; s < 2; ++s) {
          re[1] = mfma_h(pr[1][s], BC[s], s == 0 ? zero16() : re[1]);
          im[1] = mfma_h(pi[1][s], BC[s], s == 0 ? zero16() : im[1]);
          re[1] = mfma_h(pi[1][s], BN[s], re[1]);
          im[1] = mfma_h(pr[1][s], BS[s], im[1]);
        }
        twiddle_pack(TW + 8192, 1);
#pragma unroll
        for (int b = 0; b < 2; ++b)
#pragma unroll
          for (int s = 0; s < 2; ++s) {
            re[b] = mfma_h(FC[s], pr[b][s], s == 0 ? zero16() : re[b]);
            im[b] = mfma_h(FC[s], pi[b][s], s == 0 ? zero16() : im[b]);
            re[b] = mfma_h(FN[s], pi[b][s], re[b]);
            im[b] = mfma_h(FS[s], pr[b][s], im[b]);
          }
        const float osc = as_f((unsigned)(127 - 126 + 3 + ex) << 23) * ((realcol ? 1024.0f : 2048.0f) / (2048.0f * 448.0f));
        const float isc = realcol ? 0.0f : osc;
#pragma unroll
        for (int b = 0; b < 2; ++b)
#pragma unroll
          for (int i = 0; i < 16; ++i) yo[b][i] = pk_bf16(opq(re[b][i] * osc), opq(im[b][i] * isc));
      } else {
#pragma unroll
        for (int b = 0; b < 2; ++b)
#pragma unroll
          for (int i = 0; i < 16; ++i) yo[b][i] = 0u;
      }
#pragma unroll
      for (int i = 0; i < 16; ++i) {
        v2u v = {yo[0][i], yo[1][i]};
        *(LAS v2u*)(IO + ((i & 3) + 8 * (i >> 2) + 4 * h) * 256 + r * 8) = v;
      }
      wg_barrier();
      v4u yv[8];
#pragma unroll
      for (int i = 0; i < 8; ++i)
#pragma unroll
        for (int q = 0; q < 4; ++q) yv[i][q] = *(LAS unsigned*)(L + X_OFF + (4 * h + q) * 8192 + (w * 8 + i) * 128 + r * 4);
#pragma unroll
      for (int i = 0; i < 8; ++i) st_sc1(yv[i], rs_ws, lane * 16 + (i & 1) * 1024, WS_Y + ((w * 4 + (i >> 1)) * 29 + j) * 2048);
      wait_vm<0>();
      wg_barrier();
      if (tid == 0) __hip_atomic_store(wsw + (WS_F2 / 4) + (224 + j) * 32, tag, RLX_AGENT);
    } else if (j == 31) {
      poll_flags(wsw + (WS_F1 / 4) + (tid & 255) * 32, tag);
      wg_barrier();
      if (tid == 0) __hip_atomic_store(wsw + WS_BASE / 4, tag, RLX_AGENT);
    }
  }
}

}

extern "C" void kernel_launch(void* const* d_in, const int* in_sizes, int n_in, void* d_out, int out_size, void* d_ws, size_t ws_size,
                              hipStream_t stream) {
  (void)in_sizes; (void)n_in; (void)out_size; (void)ws_size;
  (void)hipFuncSetAttribute((const void*)fused_kernel, hipFuncAttributeMaxDynamicSharedMemorySize, LDS_BYTES);
  fused_kernel<<<256, 512, LDS_BYTES, stream>>>((const unsigned short*)d_in[0], (const unsigned short*)d_in[1], (const unsigned short*)d_in[2],
                                                (const unsigned short*)d_in[4], (const unsigned short*)d_in[5], (const unsigned short*)d_in[6],
                                                (const unsigned short*)d_in[7], (unsigned short*)d_out,
                                                (unsigned char*)d_ws);
}
```
